```python
import torch

def _softmax(x, axis=-1):
    return torch.softmax(x, dim=axis)
def _gelu(x):
    return torch.nn.functional.gelu(x, approximate="tanh")
def _silu(x):
    return torch.nn.functional.silu(x)
def _sigmoid(x):
    return torch.sigmoid(x)
def _layernorm(x, axis=-1, eps=1e-5):
    m = x.mean(dim=axis, keepdim=True)
    v = x.var(dim=axis, keepdim=True, unbiased=False)
    return (x - m) / torch.sqrt(v + eps)
def _scatter_add(tgt, idx, src, axis=0):
    return tgt.clone().index_add_(axis, idx, src)
def _segment_sum(src, seg, n):
    out = torch.zeros((n,) + src.shape[1:], dtype=src.dtype)
    return out.index_add_(0, seg, src)
def _pad(x, pw):
    flat = tuple(v for pair in reversed(pw) for v in pair)
    return torch.nn.functional.pad(x, flat)

kernel_name = 'sequential_preLN__ssm_conv_h3__glu_gated_reglu__prompt'

def setup_inputs():

    _w = (lambda a: (lambda t: t.to(torch.bfloat16) if t.is_floating_point() else t)(torch.as_tensor(a)))
    def _A_setup():
        B, S, D = 1, 2048, 512
        ks = 4
        return {
            "q": _w((lambda *s: torch.randn(*s, dtype=torch.bfloat16) * 0.05)(B, S, D)),
            "k": _w((lambda *s: torch.randn(*s, dtype=torch.bfloat16) * 0.2)(B, S, D)),
            "v": _w((lambda *s: torch.randn(*s, dtype=torch.bfloat16) * 0.2)(B, S, D)),
            "shift_k": _w((lambda *s: torch.randn(*s, dtype=torch.bfloat16) * 0.2)(D, ks) * 0.5),
            "ssm_k": _w((lambda *s: torch.randn(*s, dtype=torch.bfloat16) * 0.4)(D, S) * (1.0 / S ** 0.5)),
            "ssm_D": _w((lambda *s: torch.randn(*s, dtype=torch.bfloat16) * 0.2)(D)),
        }
    _A_in = _A_setup()

    def _B_setup():
        B, S, D, F = 1, 2048, 512, 1792
        return {
            "x": _w((lambda *s: torch.randn(*s, dtype=torch.bfloat16) * 0.05)(B, S, D)),
            "W_gate": _w((lambda *s: torch.randn(*s, dtype=torch.bfloat16) * 0.2)(D, F) * (2.0 / D) ** 0.5),
            "W_up": _w((lambda *s: torch.randn(*s, dtype=torch.bfloat16) * 0.2)(D, F) * (2.0 / D) ** 0.5),
            "W_down": _w((lambda *s: torch.randn(*s, dtype=torch.bfloat16) * 0.2)(F, D) * (1.0 / F) ** 0.5),
        }
    _B_in = _B_setup()

    return {
        "A_q": _A_in["q"],
        "A_k": _A_in["k"],
        "A_v": _A_in["v"],
        "A_shift_k": _A_in["shift_k"],
        "A_ssm_k": _A_in["ssm_k"],
        "A_ssm_D": _A_in["ssm_D"],
        "B_x": _B_in["x"],
        "B_W_gate": _B_in["W_gate"],
        "B_W_up": _B_in["W_up"],
        "B_W_down": _B_in["W_down"],
    }

def reference(A_q, A_k, A_v, A_shift_k, A_ssm_k, A_ssm_D, B_x, B_W_gate, B_W_up, B_W_down):
    def _A_ref(q, k, v, shift_k, ssm_k, ssm_D):
        B, S, D = q.shape
        ks = shift_k.shape[-1]
        k_pad = _pad(k, ((0, 0), (ks - 1, 0), (0, 0)))
        k_shift = torch.zeros_like(k)
        for t in range(ks):
            k_shift = k_shift + k_pad[:, t:t + S, :] * shift_k[:, ks - 1 - t][None, None, :]
        kv = k_shift * v
        L = 2 * S
        kv_pad = _pad(kv, ((0, 0), (0, S), (0, 0)))
        kf_pad = _pad(ssm_k, ((0, 0), (0, S)))
        kv_f = (lambda x, axes=(-2,-1): torch.fft.rfft2(x.float(), dim=axes))(kv_pad[:, None, :, :], axes=(1, 2))
        kf_f = (lambda x, axes=(-2,-1): torch.fft.rfft2(x.float(), dim=axes))(kf_pad[:, None, :], axes=(1, 2))
        kf_f = (lambda x, *d: x.permute(*d))(kf_f, 1, 2, 0)
        y_f = kv_f * kf_f[None, :, :, :]
        y = (lambda x, s, axes=(-2,-1): torch.fft.irfft2(x, s=s, dim=axes).to(torch.bfloat16))(y_f, (1, L), axes=(1, 2))[:, 0, :S, :]
        y = y + kv * ssm_D[None, None, :]
        return q * y

    def _B_ref(x, W_gate, W_up, W_down):
        gate = torch.einsum("bsd,df->bsf", x, W_gate)
        up = torch.einsum("bsd,df->bsf", x, W_up)
        gate = gate * (lambda x, dt: x.to(dt))(gate > 0, gate.dtype)
        h = gate * up
        return torch.einsum("bsf,fd->bsd", h, W_down)

    x = A_q
    h = x + _A_ref(_layernorm(x, axis=-1), A_k, A_v, A_shift_k, A_ssm_k, A_ssm_D)
    h2 = h + _B_ref(_layernorm(h, axis=-1), B_W_gate, B_W_up, B_W_down)
    return h2
```

```cpp
#include <hip/hip_runtime.h>
#include <hip/hip_bf16.h>

typedef unsigned short u16;
typedef unsigned int u32;
using bf16x8 = __attribute__((ext_vector_type(8))) __bf16;
using f32x4 = __attribute__((ext_vector_type(4))) float;
using f32x16 = __attribute__((ext_vector_type(16))) float;
using u32x4 = __attribute__((ext_vector_type(4))) u32;
using u32x2 = __attribute__((ext_vector_type(2))) u32;

constexpr int S_ = 2048, D_ = 512, F_ = 1792;

__device__ __forceinline__ float bf2f(u32 u) { return __uint_as_float(u << 16); }
__device__ __forceinline__ float bflo(u32 u) { return __uint_as_float(u << 16); }
__device__ __forceinline__ float bfhi(u32 u) { return __uint_as_float(u & 0xffff0000u); }
__device__ __forceinline__ u32 f2bf(float f) {
  __bf16 b = (__bf16)f;
  return (u32)__builtin_bit_cast(u16, b);
}
__device__ __forceinline__ float rbf(float f) { return bf2f(f2bf(f)); }
__device__ __forceinline__ u32 pack2(float lo, float hi) { return f2bf(lo) | (f2bf(hi) << 16); }

constexpr int KCS = 4160;
constexpr int KCH = 8 * KCS;
constexpr int UCH = 64 * 144;
constexpr int K1_KOFF = 0;
constexpr int K1_UOFF = 2 * KCH;
constexpr int K1_ROFF = K1_UOFF + 2 * UCH;
constexpr int K1_LDS = K1_ROFF + 4 * 4096;

__device__ __forceinline__ u32 rot16(u32 x) { return (x >> 16) | (x << 16); }

__global__ __launch_bounds__(256) void k1_conv(const u16* __restrict__ kin, const u16* __restrict__ vin,
                                               const u16* __restrict__ shiftk, const u16* __restrict__ ssmk,
                                               const u16* __restrict__ ssmD, const u16* __restrict__ Wg,
                                               const u16* __restrict__ Wu, const u16* __restrict__ Wd,
                                               u16* __restrict__ y2T, u16* __restrict__ Wgu_f,
                                               u16* __restrict__ Wd_f) {
  extern __shared__ __attribute__((aligned(16))) char smem[];
  const int tid = threadIdx.x;
  const int bid = blockIdx.x;
  const int g = (bid & 7) * 32 + (bid >> 3);
  const int d0 = 2 * g;

  u32 kw[11], vw[8];
  {
    const u32* kp = (const u32*)(kin + d0);
    const u32* vp = (const u32*)(vin + d0);
    const int r0 = 8 * tid;
#pragma unroll
    for (int i = 0; i < 11; ++i) {
      int r = r0 - 3 + i;
      kw[i] = (r >= 0) ? kp[(size_t)r * (D_ / 2)] : 0u;
    }
#pragma unroll
    for (int i = 0; i < 8; ++i) vw[i] = vp[(size_t)(r0 + i) * (D_ / 2)];
  }
  u32x4 g1[2], g0[2];
#pragma unroll
  for (int c = 0; c < 2; ++c) {
    const u16* kr = ssmk + (size_t)(d0 + c) * S_;
    int a = 2040 - 8 * tid;
    g1[c] = *(const u32x4*)(kr + a);
    if (a >= 8) g0[c] = *(const u32x4*)(kr + a - 8);
    else g0[c] = u32x4{0, 0, 0, 0};
  }
  if (tid < 168) {
    int blk = bid * 168 + tid;
    int which = blk / 14336;
    int rem = blk - which * 14336;
    const u16* src;
    int ld, kq, nq;
    if (which < 2) {
      kq = rem / 224; nq = rem - kq * 224; ld = F_;
      src = (which == 0 ? Wg : Wu);
    } else {
      kq = rem / 64; nq = rem - kq * 64; ld = D_;
      src = Wd;
    }
    u32x4 rws[8];
#pragma unroll
    for (int i = 0; i < 8; ++i) rws[i] = *(const u32x4*)(src + (size_t)(kq * 8 + i) * ld + nq * 8);
    u32x4 o[8];
#pragma unroll
    for (int n2 = 0; n2 < 4; ++n2) {
#pragma unroll
      for (int kk = 0; kk < 4; ++kk) {
        u32 a = rws[2 * kk][n2], b = rws[2 * kk + 1][n2];
        o[2 * n2][kk] = (a & 0xffffu) | (b << 16);
        o[2 * n2 + 1][kk] = (a >> 16) | (b & 0xffff0000u);
      }
    }
    char* dst;
    if (which < 2) {
      int fb = nq >> 1, kb = kq >> 2;
      dst = (char*)Wgu_f + (size_t)(((fb * 2 + which) * 16 + kb)) * 1024 + ((kq & 3) * 16 + (nq & 1) * 8) * 16;
    } else {
      int db = nq >> 1, kb = kq >> 2;
      dst = (char*)Wd_f + (size_t)(db * 56 + kb) * 1024 + ((kq & 3) * 16 + (nq & 1) * 8) * 16;
    }
#pragma unroll
    for (int n = 0; n < 8; ++n) *(u32x4*)(dst + n * 16) = o[n];
  }
  {
    u32x4 z = {0, 0, 0, 0};
    for (int i = tid; i < 2 * 288; i += 256) {
      int c = i / 288, j = i - c * 288;
      *(u32x4*)(smem + K1_UOFF + c * UCH + j * 16) = z;
    }
    if (tid < 64) {
      int c = tid >> 5, r = (tid >> 2) & 7, q = 256 + (tid & 3);
      *(u32x4*)(smem + K1_KOFF + c * KCH + r * KCS + q * 16) = z;
    }
  }
#pragma unroll
  for (int c = 0; c < 2; ++c) {
    u32 W[8], O[7];
#pragma unroll
    for (int i = 0; i < 4; ++i) {
      W[i] = rot16(g1[c][3 - i]);
      W[4 + i] = rot16(g0[c][3 - i]);
    }
#pragma unroll
    for (int i = 0; i < 7; ++i) O[i] = (W[i] >> 16) | (W[i + 1] << 16);
    char* base = smem + K1_KOFF + c * KCH + tid * 16;
#pragma unroll
    for (int r = 0; r < 8; ++r) {
      u32x4 val;
      if ((r & 1) == 0) val = u32x4{W[r / 2], W[r / 2 + 1], W[r / 2 + 2], W[r / 2 + 3]};
      else val = u32x4{O[r / 2], O[r / 2 + 1], O[r / 2 + 2], O[r / 2 + 3]};
      *(u32x4*)(base + r * KCS) = val;
    }
  }
  {
    const u32x2 sw0 = *(const u32x2*)(shiftk + (size_t)d0 * 4);
    const u32x2 sw1 = *(const u32x2*)(shiftk + (size_t)(d0 + 1) * 4);
    float w[2][4] = {{bflo(sw0[0]), bfhi(sw0[0]), bflo(sw0[1]), bfhi(sw0[1])},
                     {bflo(sw1[0]), bfhi(sw1[0]), bflo(sw1[1]), bfhi(sw1[1])}};
#pragma unroll
    for (int c = 0; c < 2; ++c) {
      float kf[11];
#pragma unroll
      for (int i = 0; i < 11; ++i) kf[i] = c ? bfhi(kw[i]) : bflo(kw[i]);
      float u[8];
#pragma unroll
      for (int i = 0; i < 8; ++i) {
        float acc = rbf(kf[i] * w[c][3]);
        acc = rbf(acc + rbf(kf[i + 1] * w[c][2]));
        acc = rbf(acc + rbf(kf[i + 2] * w[c][1]));
        acc = rbf(acc + rbf(kf[i + 3] * w[c][0]));
        float vf = c ? bfhi(vw[i]) : bflo(vw[i]);
        u[i] = acc * vf;
      }
      u32x4 pk = {pack2(u[0], u[1]), pack2(u[2], u[3]), pack2(u[4], u[5]), pack2(u[6], u[7])};
      *(u32x4*)(smem + K1_UOFF + c * UCH + (32 + (tid >> 3)) * 144 + (tid & 7) * 16) = pk;
    }
  }
  __syncthreads();

  const int wave = tid >> 6, lane = tid & 63;
  const int c = wave >> 1, hf = wave & 1;
  const int b = lane & 31, h = lane >> 5;
  f32x16 acc0, acc1;
#pragma unroll
  for (int i = 0; i < 16; ++i) { acc0[i] = 0.f; acc1[i] = 0.f; }
  {
    const int t0 = -16 + 1024 * hf;
    const int i0 = 2047 - t0 - b + 8 * h;
    const int r = i0 & 7;
    const char* ap = smem + K1_KOFF + c * KCH + r * KCS + (i0 - r) * 2;
    const char* ub = smem + K1_UOFF + c * UCH + (32 + b) * 144 + 16 * h;
    auto bload = [&](int o) -> bf16x8 {
      int base = -o;
      int blkoff = base >> 6;
      int e0 = base & 63;
      return *(const bf16x8*)(ub + blkoff * 144 + e0 * 2);
    };
    bf16x8 bm2 = bload(t0 - 32), bm1 = bload(t0 - 16);
#pragma unroll
    for (int i = 0; i < 64; ++i) {
      bf16x8 a = *(const bf16x8*)(ap - 32 * i);
      bf16x8 bn = bload(t0 + 16 * i);
      acc0 = __builtin_amdgcn_mfma_f32_32x32x16_bf16(a, bn, acc0, 0, 0, 0);
      acc1 = __builtin_amdgcn_mfma_f32_32x32x16_bf16(a, bm2, acc1, 0, 0, 0);
      bm2 = bm1;
      bm1 = bn;
    }
  }
  {
    float* mine = (float*)(smem + K1_ROFF + wave * 4096);
    const float* other = (const float*)(smem + K1_ROFF + (wave ^ 1) * 4096);
#pragma unroll
    for (int i = 0; i < 4; ++i) {
      f32x4 x;
#pragma unroll
      for (int j = 0; j < 4; ++j) x[j] = hf ? acc0[4 * i + j] : acc1[4 * i + j];
      *(f32x4*)(mine + (i * 64 + lane) * 4) = x;
    }
    __syncthreads();
    f32x16 acc = hf ? acc1 : acc0;
#pragma unroll
    for (int i = 0; i < 4; ++i) {
      f32x4 x = *(const f32x4*)(other + (i * 64 + lane) * 4);
#pragma unroll
      for (int j = 0; j < 4; ++j) acc[4 * i + j] += x[j];
    }
    const float dval = bf2f(ssmD[d0 + c]);
    u16* orow = y2T + (size_t)(d0 + c) * S_;
#pragma unroll
    for (int i = 0; i < 4; ++i) {
      int e = 32 * hf + 8 * i + 4 * h;
      u32x2 kvp = *(const u32x2*)(smem + K1_UOFF + c * UCH + (32 + b) * 144 + e * 2);
      float kv[4] = {bflo(kvp[0]), bfhi(kvp[0]), bflo(kvp[1]), bfhi(kvp[1])};
      float o[4];
#pragma unroll
      for (int j = 0; j < 4; ++j) o[j] = rbf(acc[4 * i + j]) + rbf(kv[j] * dval);
      u32x2 pk = {pack2(o[0], o[1]), pack2(o[2], o[3])};
      *(u32x2*)(orow + 64 * b + e) = pk;
    }
  }
}

__device__ __forceinline__ float wave_sum(float x) {
#pragma unroll
  for (int o = 32; o > 0; o >>= 1) x += __shfl_xor(x, o, 64);
  return x;
}

__device__ __forceinline__ void ln_bf16(const float (&x)[8], float (&o)[8]) {
  float s = 0.f;
#pragma unroll
  for (int i = 0; i < 8; ++i) s += x[i];
  s = wave_sum(s);
  const float mean = s * (1.f / 512.f);
  float ss = 0.f;
#pragma unroll
  for (int i = 0; i < 8; ++i) { float dlt = x[i] - mean; ss += dlt * dlt; }
  ss = wave_sum(ss);
  const float mb = rbf(mean);
  const float vb = rbf(ss * (1.f / 512.f));
  const float sd = rbf(sqrtf(rbf(vb + 1e-5f)));
#pragma unroll
  for (int i = 0; i < 8; ++i) o[i] = rbf(rbf(x[i] - mb) / sd);
}

__global__ __launch_bounds__(256) void k1b_rows(const u16* __restrict__ q, const u16* __restrict__ y2T,
                                                u16* __restrict__ hout, u16* __restrict__ hln_f) {
  __shared__ __attribute__((aligned(16))) u32 tile[8][264];
  const int tid = threadIdx.x;
  const int s0 = blockIdx.x * 8;
  const int wave = tid >> 6, lane = tid & 63;
  u32x4 ya = *(const u32x4*)(y2T + (size_t)(2 * tid) * S_ + s0);
  u32x4 yb = *(const u32x4*)(y2T + (size_t)(2 * tid + 1) * S_ + s0);
  u32x4 qv[2];
#pragma unroll
  for (int r = 0; r < 2; ++r) qv[r] = *(const u32x4*)(q + (size_t)(s0 + 2 * wave + r) * D_ + lane * 8);
#pragma unroll
  for (int i = 0; i < 4; ++i) {
    tile[2 * i][tid] = (ya[i] & 0xffffu) | (yb[i] << 16);
    tile[2 * i + 1][tid] = (ya[i] >> 16) | (yb[i] & 0xffff0000u);
  }
  __syncthreads();
#pragma unroll
  for (int r = 0; r < 2; ++r) {
    const int row = 2 * wave + r;
    const int s = s0 + row;
    u32x4 yv = *(const u32x4*)(&tile[row][lane * 4]);
    float x[8], y[8], ql[8], hh[8], hl[8];
#pragma unroll
    for (int i = 0; i < 4; ++i) {
      x[2 * i] = bflo(qv[r][i]); x[2 * i + 1] = bfhi(qv[r][i]);
      y[2 * i] = bflo(yv[i]); y[2 * i + 1] = bfhi(yv[i]);
    }
    ln_bf16(x, ql);
#pragma unroll
    for (int i = 0; i < 8; ++i) hh[i] = rbf(x[i] + rbf(ql[i] * y[i]));
    ln_bf16(hh, hl);
    u32x4 hp = {pack2(hh[0], hh[1]), pack2(hh[2], hh[3]), pack2(hh[4], hh[5]), pack2(hh[6], hh[7])};
    u32x4 lp = {pack2(hl[0], hl[1]), pack2(hl[2], hl[3]), pack2(hl[4], hl[5]), pack2(hl[6], hl[7])};
    *(u32x4*)(hout + (size_t)s * D_ + lane * 8) = hp;
    char* dst = (char*)hln_f + (size_t)(((s >> 7) * 16 + (lane >> 2)) * 8 + ((s >> 4) & 7)) * 1024 +
                ((lane & 3) * 16 + (s & 15)) * 16;
    *(u32x4*)dst = lp;
  }
}

__global__ __launch_bounds__(256) void k2_gemm1(const u16* __restrict__ hln_f, const u16* __restrict__ Wgu_f,
                                                u16* __restrict__ hh_f) {
  extern __shared__ __attribute__((aligned(16))) char smem[];
  const int tid = threadIdx.x;
  const int wave = tid >> 6, lane = tid & 63;
  const int bid = blockIdx.x;
  const int xcd = bid & 7, idx = bid >> 3;
  const int ft = (xcd & 1) * 7 + (idx % 7);
  const int rb = (xcd >> 1) * 4 + (idx / 7);

  {
    const char* src = (const char*)hln_f + (size_t)rb * 131072 + lane * 16;
#pragma unroll
    for (int i = 0; i < 32; ++i) {
      int fr = i * 4 + wave;
      __builtin_amdgcn_global_load_lds((const u32*)(src + fr * 1024), (u32*)(smem + fr * 1024), 16, 0, 0);
    }
  }
  const char* wbase = (const char*)Wgu_f + (size_t)((ft * 8 + wave * 2) * 2) * 16 * 1024 + lane * 16;
  constexpr int PF = 3;
  bf16x8 wf[PF][4];
#pragma unroll
  for (int p = 0; p < PF; ++p)
#pragma unroll
    for (int j = 0; j < 4; ++j) wf[p][j] = *(const bf16x8*)(wbase + (j * 16 + p) * 1024);

  f32x4 acc[4][8];
#pragma unroll
  for (int j = 0; j < 4; ++j)
#pragma unroll
    for (int m = 0; m < 8; ++m) acc[j][m] = f32x4{0.f, 0.f, 0.f, 0.f};

  asm volatile("s_waitcnt vmcnt(0)" ::: "memory");
  __syncthreads();

#pragma unroll
  for (int kb = 0; kb < 16; ++kb) {
    bf16x8 xf[8];
#pragma unroll
    for (int m = 0; m < 8; ++m) xf[m] = *(const bf16x8*)(smem + (kb * 8 + m) * 1024 + lane * 16);
    bf16x8 wc[4];
#pragma unroll
    for (int j = 0; j < 4; ++j) wc[j] = wf[kb % PF][j];
    if (kb + PF < 16) {
#pragma unroll
      for (int j = 0; j < 4; ++j) wf[kb % PF][j] = *(const bf16x8*)(wbase + (j * 16 + kb + PF) * 1024);
    }
#pragma unroll
    for (int j = 0; j < 4; ++j)
#pragma unroll
      for (int m = 0; m < 8; ++m)
        acc[j][m] = __builtin_amdgcn_mfma_f32_16x16x32_bf16(wc[j], xf[m], acc[j][m], 0, 0, 0);
  }
  const int qd = lane >> 4, sl = lane & 15;
  char* obase = (char*)hh_f + (size_t)(ft * 4 + wave) * 1024 + ((qd >> 1) * 16 + sl) * 16 + (qd & 1) * 8;
#pragma unroll
  for (int fbl = 0; fbl < 2; ++fbl)
#pragma unroll
    for (int m = 0; m < 8; ++m) {
      float o[4];
#pragma unroll
      for (int r = 0; r < 4; ++r) {
        float gt = rbf(acc[fbl * 2][m][r]);
        float up = rbf(acc[fbl * 2 + 1][m][r]);
        gt = gt * (gt > 0.f ? 1.f : 0.f);
        o[r] = gt * up;
      }
      u32x2 pk = {pack2(o[0], o[1]), pack2(o[2], o[3])};
      *(u32x2*)(obase + (size_t)(rb * 8 + m) * 56 * 1024 + fbl * 512) = pk;
    }
}

__global__ __launch_bounds__(256) void k3_gemm2(const u16* __restrict__ hh_f, const u16* __restrict__ Wd_f,
                                                const u16* __restrict__ hin, u16* __restrict__ out) {
  __shared__ __attribute__((aligned(16))) char red[4 * 16 * 1024];
  const int tid = threadIdx.x;
  const int wave = tid >> 6, lane = tid & 63;
  const int bid = blockIdx.x;
  const int xcd = bid & 7, idx = bid >> 3;
  const int dt = idx & 7, st = xcd * 4 + (idx >> 3);

  const char* wb = (const char*)Wd_f + (size_t)(dt * 4 * 56 + wave * 14) * 1024 + lane * 16;
  const char* xb = (const char*)hh_f + (size_t)(st * 4 * 56 + wave * 14) * 1024 + lane * 16;
  constexpr int PF = 3;
  bf16x8 wf[PF][4], xf[PF][4];
#pragma unroll
  for (int p = 0; p < PF; ++p)
#pragma unroll
    for (int j = 0; j < 4; ++j) {
      wf[p][j] = *(const bf16x8*)(wb + (j * 56 + p) * 1024);
      xf[p][j] = *(const bf16x8*)(xb + (j * 56 + p) * 1024);
    }
  f32x4 acc[4][4];
#pragma unroll
  for (int i = 0; i < 4; ++i)
#pragma unroll
    for (int j = 0; j < 4; ++j) acc[i][j] = f32x4{0.f, 0.f, 0.f, 0.f};
#pragma unroll
  for (int kb = 0; kb < 14; ++kb) {
    bf16x8 wc[4], xc[4];
#pragma unroll
    for (int j = 0; j < 4; ++j) { wc[j] = wf[kb % PF][j]; xc[j] = xf[kb % PF][j]; }
    if (kb + PF < 14) {
#pragma unroll
      for (int j = 0; j < 4; ++j) {
        wf[kb % PF][j] = *(const bf16x8*)(wb + (j * 56 + kb + PF) * 1024);
        xf[kb % PF][j] = *(const bf16x8*)(xb + (j * 56 + kb + PF) * 1024);
      }
    }
#pragma unroll
    for (int i = 0; i < 4; ++i)
#pragma unroll
      for (int j = 0; j < 4; ++j)
        acc[i][j] = __builtin_amdgcn_mfma_f32_16x16x32_bf16(wc[i], xc[j], acc[i][j], 0, 0, 0);
  }
  const int qd = lane >> 4, sl = lane & 15;
  u32x2 hv[4];
#pragma unroll
  for (int j = 0; j < 4; ++j)
    hv[j] = *(const u32x2*)(hin + (size_t)(st * 64 + j * 16 + sl) * D_ + dt * 64 + wave * 16 + qd * 4);
#pragma unroll
  for (int i = 0; i < 4; ++i)
#pragma unroll
    for (int j = 0; j < 4; ++j)
      *(f32x4*)(red + ((wave * 4 + i) * 4 + j) * 1024 + lane * 16) = acc[i][j];
  __syncthreads();
#pragma unroll
  for (int j = 0; j < 4; ++j) {
    f32x4 sum = f32x4{0.f, 0.f, 0.f, 0.f};
#pragma unroll
    for (int w = 0; w < 4; ++w) sum += *(const f32x4*)(red + ((w * 4 + wave) * 4 + j) * 1024 + lane * 16);
    float hx[4] = {bflo(hv[j][0]), bfhi(hv[j][0]), bflo(hv[j][1]), bfhi(hv[j][1])};
    float o[4];
#pragma unroll
    for (int r = 0; r < 4; ++r) o[r] = hx[r] + rbf(sum[r]);
    u32x2 pk = {pack2(o[0], o[1]), pack2(o[2], o[3])};
    *(u32x2*)(out + (size_t)(st * 64 + j * 16 + sl) * D_ + dt * 64 + wave * 16 + qd * 4) = pk;
  }
}

extern "C" void kernel_launch(void* const* d_in, const int* in_sizes, int n_in, void* d_out, int out_size,
                              void* d_ws, size_t ws_size, hipStream_t stream) {
  const u16* q = (const u16*)d_in[0];
  const u16* k = (const u16*)d_in[1];
  const u16* v = (const u16*)d_in[2];
  const u16* shiftk = (const u16*)d_in[3];
  const u16* ssmk = (const u16*)d_in[4];
  const u16* ssmD = (const u16*)d_in[5];
  const u16* Wg = (const u16*)d_in[7];
  const u16* Wu = (const u16*)d_in[8];
  const u16* Wd = (const u16*)d_in[9];
  char* ws = (char*)d_ws;
  u16* y2T = (u16*)(ws);
  u16* hbuf = (u16*)(ws + (2u << 20));
  u16* hln_f = (u16*)(ws + (4u << 20));
  u16* Wgu_f = (u16*)(ws + (6u << 20));
  u16* Wd_f = (u16*)(ws + (10u << 20));
  u16* hh_f = (u16*)(ws + (12u << 20));

  hipFuncSetAttribute((const void*)k1_conv, hipFuncAttributeMaxDynamicSharedMemorySize, K1_LDS);
  hipFuncSetAttribute((const void*)k2_gemm1, hipFuncAttributeMaxDynamicSharedMemorySize, 131072);

  k1_conv<<<256, 256, K1_LDS, stream>>>(k, v, shiftk, ssmk, ssmD, Wg, Wu, Wd, y2T, Wgu_f, Wd_f);
  k1b_rows<<<256, 256, 0, stream>>>(q, y2T, hbuf, hln_f);
  k2_gemm1<<<224, 256, 131072, stream>>>(hln_f, Wgu_f, hh_f);
  k3_gemm2<<<256, 256, 0, stream>>>(hh_f, Wd_f, hbuf, (u16*)d_out);
}
```

```cpp
#include <hip/hip_runtime.h>
#include <hip/hip_bf16.h>

typedef unsigned short u16;
typedef unsigned int u32;
using bf16x8 = __attribute__((ext_vector_type(8))) __bf16;
using f32x4 = __attribute__((ext_vector_type(4))) float;
using f32x16 = __attribute__((ext_vector_type(16))) float;
using u32x4 = __attribute__((ext_vector_type(4))) u32;
using u32x2 = __attribute__((ext_vector_type(2))) u32;

constexpr int S_ = 2048, D_ = 512, F_ = 1792;

#define STAMPW(kid, idx)

__device__ __forceinline__ float bf2f(u32 u) { return __uint_as_float(u << 16); }
__device__ __forceinline__ float bflo(u32 u) { return __uint_as_float(u << 16); }
__device__ __forceinline__ float bfhi(u32 u) { return __uint_as_float(u & 0xffff0000u); }
__device__ __forceinline__ u32 f2bf(float f) {
  __bf16 b = (__bf16)f;
  return (u32)__builtin_bit_cast(u16, b);
}
__device__ __forceinline__ float rbf(float f) { return bf2f(f2bf(f)); }
__device__ __forceinline__ u32 pack2(float lo, float hi) { return f2bf(lo) | (f2bf(hi) << 16); }

__global__ __launch_bounds__(256) void k0_pre(const u16* __restrict__ kin, const u16* __restrict__ vin,
                                              const u16* __restrict__ shiftk, const u16* __restrict__ Wg,
                                              const u16* __restrict__ Wu, const u16* __restrict__ Wd,
                                              u16* __restrict__ kvT, u16* __restrict__ Wgu_f,
                                              u16* __restrict__ Wd_f) {
  const int tid = threadIdx.x;
  const int bid = blockIdx.x;
  STAMPW(0, 0);
  const int ct = bid & 7, rt = bid >> 3;
  const int d0 = ct * 64 + (tid & 31) * 2;
  const int r0 = rt * 64 + (tid >> 5) * 8;
  u32 kw[11], vw[8];
  {
    const u32* kp = (const u32*)(kin + d0);
    const u32* vp = (const u32*)(vin + d0);
#pragma unroll
    for (int i = 0; i < 11; ++i) {
      int r = r0 - 3 + i;
      kw[i] = (r >= 0) ? kp[(size_t)r * (D_ / 2)] : 0u;
    }
#pragma unroll
    for (int i = 0; i < 8; ++i) vw[i] = vp[(size_t)(r0 + i) * (D_ / 2)];
  }
  const u32x2 sw0 = *(const u32x2*)(shiftk + (size_t)d0 * 4);
  const u32x2 sw1 = *(const u32x2*)(shiftk + (size_t)(d0 + 1) * 4);
  if (tid < 168) {
    int blk = bid * 168 + tid;
    int which = blk / 14336;
    int rem = blk - which * 14336;
    const u16* src;
    int ld, kq, nq;
    if (which < 2) {
      kq = rem / 224; nq = rem - kq * 224; ld = F_;
      src = (which == 0 ? Wg : Wu);
    } else {
      kq = rem / 64; nq = rem - kq * 64; ld = D_;
      src = Wd;
    }
    u32x4 rws[8];
#pragma unroll
    for (int i = 0; i < 8; ++i) rws[i] = *(const u32x4*)(src + (size_t)(kq * 8 + i) * ld + nq * 8);
    u32x4 o[8];
#pragma unroll
    for (int n2 = 0; n2 < 4; ++n2) {
#pragma unroll
      for (int kk = 0; kk < 4; ++kk) {
        u32 a = rws[2 * kk][n2], b = rws[2 * kk + 1][n2];
        o[2 * n2][kk] = (a & 0xffffu) | (b << 16);
        o[2 * n2 + 1][kk] = (a >> 16) | (b & 0xffff0000u);
      }
    }
    char* dst;
    if (which < 2) {
      int fb = nq >> 1, kb = kq >> 2;
      dst = (char*)Wgu_f + (size_t)(((fb * 2 + which) * 16 + kb)) * 1024 + ((kq & 3) * 16 + (nq & 1) * 8) * 16;
    } else {
      int db = nq >> 1, kb = kq >> 2;
      dst = (char*)Wd_f + (size_t)(db * 56 + kb) * 1024 + ((kq & 3) * 16 + (nq & 1) * 8) * 16;
    }
#pragma unroll
    for (int n = 0; n < 8; ++n) *(u32x4*)(dst + n * 16) = o[n];
  }
  {
    float w[2][4] = {{bflo(sw0[0]), bfhi(sw0[0]), bflo(sw0[1]), bfhi(sw0[1])},
                     {bflo(sw1[0]), bfhi(sw1[0]), bflo(sw1[1]), bfhi(sw1[1])}};
#pragma unroll
    for (int c = 0; c < 2; ++c) {
      float kf[11];
#pragma unroll
      for (int i = 0; i < 11; ++i) kf[i] = c ? bfhi(kw[i]) : bflo(kw[i]);
      float u[8];
#pragma unroll
      for (int i = 0; i < 8; ++i) {
        float acc = rbf(kf[i] * w[c][3]);
        acc = rbf(acc + rbf(kf[i + 1] * w[c][2]));
        acc = rbf(acc + rbf(kf[i + 2] * w[c][1]));
        acc = rbf(acc + rbf(kf[i + 3] * w[c][0]));
        float vf = c ? bfhi(vw[i]) : bflo(vw[i]);
        u[i] = acc * vf;
      }
      u32x4 pk = {pack2(u[0], u[1]), pack2(u[2], u[3]), pack2(u[4], u[5]), pack2(u[6], u[7])};
      *(u32x4*)(kvT + (size_t)(d0 + c) * S_ + r0) = pk;
    }
  }
  STAMPW(0, 1);
}

constexpr int KCS = 4160;
constexpr int KCH = 8 * KCS;
constexpr int UCH = 64 * 144;
constexpr int K1_KOFF = 0;
constexpr int K1_UOFF = 2 * KCH;
constexpr int K1_ROFF = K1_UOFF + 2 * UCH;
constexpr int K1_LDS = K1_ROFF + 4 * 4096;

__device__ __forceinline__ u32 rot16(u32 x) { return (x >> 16) | (x << 16); }

__global__ __launch_bounds__(256) void k1_conv(const u16* __restrict__ kvT, const u16* __restrict__ ssmk,
                                               const u16* __restrict__ ssmD, u16* __restrict__ y2T) {
  extern __shared__ __attribute__((aligned(16))) char smem[];
  const int tid = threadIdx.x;
  const int d0 = 2 * blockIdx.x;
  STAMPW(1, 0);
  u32x4 g1[2], g0[2], uv[2];
#pragma unroll
  for (int c = 0; c < 2; ++c) {
    const u16* kr = ssmk + (size_t)(d0 + c) * S_;
    int a = 2040 - 8 * tid;
    g1[c] = *(const u32x4*)(kr + a);
    if (a >= 8) g0[c] = *(const u32x4*)(kr + a - 8);
    else g0[c] = u32x4{0, 0, 0, 0};
    uv[c] = *(const u32x4*)(kvT + (size_t)(d0 + c) * S_ + tid * 8);
  }
  const float dval0 = bf2f(ssmD[d0]), dval1 = bf2f(ssmD[d0 + 1]);
  {
    u32x4 z = {0, 0, 0, 0};
    for (int i = tid; i < 2 * 288; i += 256) {
      int c = i / 288, j = i - c * 288;
      *(u32x4*)(smem + K1_UOFF + c * UCH + j * 16) = z;
    }
    if (tid < 64) {
      int c = tid >> 5, r = (tid >> 2) & 7, q = 256 + (tid & 3);
      *(u32x4*)(smem + K1_KOFF + c * KCH + r * KCS + q * 16) = z;
    }
  }
  STAMPW(1, 1);
#pragma unroll
  for (int c = 0; c < 2; ++c) {
    u32 W[8], O[7];
#pragma unroll
    for (int i = 0; i < 4; ++i) {
      W[i] = rot16(g1[c][3 - i]);
      W[4 + i] = rot16(g0[c][3 - i]);
    }
#pragma unroll
    for (int i = 0; i < 7; ++i) O[i] = (W[i] >> 16) | (W[i + 1] << 16);
    char* base = smem + K1_KOFF + c * KCH + tid * 16;
#pragma unroll
    for (int r = 0; r < 8; ++r) {
      u32x4 val;
      if ((r & 1) == 0) val = u32x4{W[r / 2], W[r / 2 + 1], W[r / 2 + 2], W[r / 2 + 3]};
      else val = u32x4{O[r / 2], O[r / 2 + 1], O[r / 2 + 2], O[r / 2 + 3]};
      *(u32x4*)(base + r * KCS) = val;
    }
    *(u32x4*)(smem + K1_UOFF + c * UCH + (32 + (tid >> 3)) * 144 + (tid & 7) * 16) = uv[c];
  }
  __syncthreads();
  STAMPW(1, 2);

  const int wave = tid >> 6, lane = tid & 63;
  const int c = wave >> 1, hf = wave & 1;
  const int b = lane & 31, h = lane >> 5;
  f32x16 acc0, acc1;
#pragma unroll
  for (int i = 0; i < 16; ++i) { acc0[i] = 0.f; acc1[i] = 0.f; }
  {
    const int i0 = 2047 - (-16 + 1024 * hf) - b + 8 * h;
    const int r = i0 & 7;
    const char* ap = smem + K1_KOFF + c * KCH + r * KCS + (i0 - r) * 2 - 32 * 63;
    const char* ub = smem + K1_UOFF + c * UCH + (32 + b - 16 * hf - 16) * 144 + 16 * h;
    auto boff = [](int j) -> int {
      int base = 16 - 16 * j;
      int blkoff = (base >= 0) ? (base >> 6) : -((-base + 63) >> 6);
      int e0 = base - blkoff * 64;
      return (blkoff + 16) * 144 + e0 * 2;
    };
    constexpr int PD = 3;
    bf16x8 af[PD + 1], bfr[PD + 3];
#pragma unroll
    for (int j = -2; j < PD; ++j) bfr[(j + 2) % (PD + 3)] = *(const bf16x8*)(ub + boff(j));
#pragma unroll
    for (int i = 0; i < PD; ++i) af[i % (PD + 1)] = *(const bf16x8*)(ap + 32 * (63 - i));
#pragma unroll
    for (int i = 0; i < 64; ++i) {
      if (i + PD < 64) {
        af[(i + PD) % (PD + 1)] = *(const bf16x8*)(ap + 32 * (63 - i - PD));
        bfr[(i + PD + 2) % (PD + 3)] = *(const bf16x8*)(ub + boff(i + PD));
      }
      __builtin_amdgcn_sched_barrier(0);
      acc0 = __builtin_amdgcn_mfma_f32_32x32x16_bf16(af[i % (PD + 1)], bfr[(i + 2) % (PD + 3)], acc0, 0, 0, 0);
      acc1 = __builtin_amdgcn_mfma_f32_32x32x16_bf16(af[i % (PD + 1)], bfr[i % (PD + 3)], acc1, 0, 0, 0);
      __builtin_amdgcn_sched_barrier(0);
    }
  }
  STAMPW(1, 3);
  {
    float* mine = (float*)(smem + K1_ROFF + wave * 4096);
    const float* other = (const float*)(smem + K1_ROFF + (wave ^ 1) * 4096);
#pragma unroll
    for (int i = 0; i < 4; ++i) {
      f32x4 x;
#pragma unroll
      for (int j = 0; j < 4; ++j) x[j] = hf ? acc0[4 * i + j] : acc1[4 * i + j];
      *(f32x4*)(mine + (i * 64 + lane) * 4) = x;
    }
    __syncthreads();
    f32x16 acc = hf ? acc1 : acc0;
#pragma unroll
    for (int i = 0; i < 4; ++i) {
      f32x4 x = *(const f32x4*)(other + (i * 64 + lane) * 4);
#pragma unroll
      for (int j = 0; j < 4; ++j) acc[4 * i + j] += x[j];
    }
    const float dval = c ? dval1 : dval0;
    u16* orow = y2T + (size_t)(d0 + c) * S_;
#pragma unroll
    for (int i = 0; i < 4; ++i) {
      int e = 32 * hf + 8 * i + 4 * h;
      u32x2 kvp = *(const u32x2*)(smem + K1_UOFF + c * UCH + (32 + b) * 144 + e * 2);
      float kv[4] = {bflo(kvp[0]), bfhi(kvp[0]), bflo(kvp[1]), bfhi(kvp[1])};
      float o[4];
#pragma unroll
      for (int j = 0; j < 4; ++j) o[j] = rbf(acc[4 * i + j]) + rbf(kv[j] * dval);
      u32x2 pk = {pack2(o[0], o[1]), pack2(o[2], o[3])};
      *(u32x2*)(orow + 64 * b + e) = pk;
    }
  }
  STAMPW(1, 4);
}

template <int CTRL>
__device__ __forceinline__ float dpp_add(float x) {
  int y = __builtin_amdgcn_update_dpp(0, __float_as_int(x), CTRL, 0xf, 0xf, true);
  return x + __int_as_float(y);
}
__device__ __forceinline__ float wave_sum(float x) {
  x = dpp_add<0xB1>(x);
  x = dpp_add<0x4E>(x);
  x = dpp_add<0x141>(x);
  x = dpp_add<0x140>(x);
  int xi = __float_as_int(x);
  float a = __int_as_float(__builtin_amdgcn_readlane(xi, 0));
  float b = __int_as_float(__builtin_amdgcn_readlane(xi, 16));
  float c = __int_as_float(__builtin_amdgcn_readlane(xi, 32));
  float d = __int_as_float(__builtin_amdgcn_readlane(xi, 48));
  return (a + b) + (c + d);
}

__device__ __forceinline__ void ln_bf16x2(const float (&x)[2][8], float (&o)[2][8]) {
  float s[2], ss[2], mean[2];
#pragma unroll
  for (int r = 0; r < 2; ++r) {
    s[r] = ((x[r][0] + x[r][1]) + (x[r][2] + x[r][3])) + ((x[r][4] + x[r][5]) + (x[r][6] + x[r][7]));
  }
#pragma unroll
  for (int r = 0; r < 2; ++r) mean[r] = wave_sum(s[r]) * (1.f / 512.f);
#pragma unroll
  for (int r = 0; r < 2; ++r) {
    ss[r] = 0.f;
#pragma unroll
    for (int i = 0; i < 8; ++i) { float dlt = x[r][i] - mean[r]; ss[r] += dlt * dlt; }
  }
#pragma unroll
  for (int r = 0; r < 2; ++r) {
    const float var = wave_sum(ss[r]) * (1.f / 512.f);
    const float mb = rbf(mean[r]);
    const float vb = rbf(var);
    const float sd = rbf(sqrtf(rbf(vb + 1e-5f)));
    const float rc = 1.0f / sd;
#pragma unroll
    for (int i = 0; i < 8; ++i) {
      float a = rbf(x[r][i] - mb);
      float qt = a * rc;
      float rem = __builtin_fmaf(-qt, sd, a);
      qt = __builtin_fmaf(rem, rc, qt);
      o[r][i] = rbf(qt);
    }
  }
}

__global__ __launch_bounds__(256) void k1b_rows(const u16* __restrict__ q, const u16* __restrict__ y2T,
                                                u16* __restrict__ hout, u16* __restrict__ hln_f) {
  __shared__ __attribute__((aligned(16))) u32 tile[8][264];
  const int tid = threadIdx.x;
  const int s0 = blockIdx.x * 8;
  const int wave = tid >> 6, lane = tid & 63;
  STAMPW(2, 0);
  u32x4 ya = *(const u32x4*)(y2T + (size_t)(2 * tid) * S_ + s0);
  u32x4 yb = *(const u32x4*)(y2T + (size_t)(2 * tid + 1) * S_ + s0);
  u32x4 qv[2];
#pragma unroll
  for (int r = 0; r < 2; ++r) qv[r] = *(const u32x4*)(q + (size_t)(s0 + 2 * wave + r) * D_ + lane * 8);
  STAMPW(2, 1);
#pragma unroll
  for (int i = 0; i < 4; ++i) {
    tile[2 * i][tid] = (ya[i] & 0xffffu) | (yb[i] << 16);
    tile[2 * i + 1][tid] = (ya[i] >> 16) | (yb[i] & 0xffff0000u);
  }
  float x[2][8], ql[2][8];
#pragma unroll
  for (int r = 0; r < 2; ++r)
#pragma unroll
    for (int i = 0; i < 4; ++i) {
      x[r][2 * i] = bflo(qv[r][i]);
      x[r][2 * i + 1] = bfhi(qv[r][i]);
    }
  ln_bf16x2(x, ql);
  __syncthreads();
  float hh[2][8], hl[2][8];
#pragma unroll
  for (int r = 0; r < 2; ++r) {
    u32x4 yv = *(const u32x4*)(&tile[2 * wave + r][lane * 4]);
#pragma unroll
    for (int i = 0; i < 4; ++i) {
      hh[r][2 * i] = rbf(x[r][2 * i] + rbf(ql[r][2 * i] * bflo(yv[i])));
      hh[r][2 * i + 1] = rbf(x[r][2 * i + 1] + rbf(ql[r][2 * i + 1] * bfhi(yv[i])));
    }
  }
  ln_bf16x2(hh, hl);
#pragma unroll
  for (int r = 0; r < 2; ++r) {
    const int s = s0 + 2 * wave + r;
    u32x4 hp = {pack2(hh[r][0], hh[r][1]), pack2(hh[r][2], hh[r][3]), pack2(hh[r][4], hh[r][5]),
                pack2(hh[r][6], hh[r][7])};
    u32x4 lp = {pack2(hl[r][0], hl[r][1]), pack2(hl[r][2], hl[r][3]), pack2(hl[r][4], hl[r][5]),
                pack2(hl[r][6], hl[r][7])};
    *(u32x4*)(hout + (size_t)s * D_ + lane * 8) = hp;
    char* dst = (char*)hln_f + (size_t)(((s >> 7) * 16 + (lane >> 2)) * 8 + ((s >> 4) & 7)) * 1024 +
                ((lane & 3) * 16 + (s & 15)) * 16;
    *(u32x4*)dst = lp;
  }
  STAMPW(2, 2);
}

__global__ __launch_bounds__(256) void k2_gemm1(const u16* __restrict__ hln_f, const u16* __restrict__ Wgu_f,
                                                u16* __restrict__ hh_f) {
  extern __shared__ __attribute__((aligned(16))) char smem[];
  const int tid = threadIdx.x;
  const int wave = tid >> 6, lane = tid & 63;
  const int bid = blockIdx.x;
  const int xcd = bid & 7, idx = bid >> 3;
  const int ft = (xcd & 1) * 7 + (idx % 7);
  const int rb = (xcd >> 1) * 4 + (idx / 7);

  STAMPW(3, 0);
  const char* wbase = (const char*)Wgu_f + (size_t)((ft * 8 + wave * 2) * 2) * 16 * 1024 + lane * 16;
  constexpr int PF = 4;
  bf16x8 wf[PF + 1][4];
#pragma unroll
  for (int p = 0; p < PF; ++p)
#pragma unroll
    for (int j = 0; j < 4; ++j) wf[p][j] = *(const bf16x8*)(wbase + (j * 16 + p) * 1024);
  {
    const char* src = (const char*)hln_f + (size_t)rb * 131072 + lane * 16;
#pragma unroll
    for (int i = 0; i < 32; ++i) {
      int fr = i * 4 + wave;
      __builtin_amdgcn_global_load_lds((const u32*)(src + fr * 1024), (u32*)(smem + fr * 1024), 16, 0, 0);
    }
  }

  f32x4 acc[4][8];
#pragma unroll
  for (int j = 0; j < 4; ++j)
#pragma unroll
    for (int m = 0; m < 8; ++m) acc[j][m] = f32x4{0.f, 0.f, 0.f, 0.f};

  asm volatile("s_waitcnt vmcnt(0)" ::: "memory");
  __syncthreads();
  STAMPW(3, 1);

  bf16x8 xf[2][8];
#pragma unroll
  for (int m = 0; m < 8; ++m) xf[0][m] = *(const bf16x8*)(smem + m * 1024 + lane * 16);
#pragma unroll
  for (int kb = 0; kb < 16; ++kb) {
    if (kb + PF < 16) {
#pragma unroll
      for (int j = 0; j < 4; ++j) wf[(kb + PF) % (PF + 1)][j] = *(const bf16x8*)(wbase + (j * 16 + kb + PF) * 1024);
    }
    if (kb + 1 < 16) {
#pragma unroll
      for (int m = 0; m < 8; ++m)
        xf[(kb + 1) & 1][m] = *(const bf16x8*)(smem + ((kb + 1) * 8 + m) * 1024 + lane * 16);
    }
    __builtin_amdgcn_sched_barrier(0);
#pragma unroll
    for (int j = 0; j < 4; ++j)
#pragma unroll
      for (int m = 0; m < 8; ++m)
        acc[j][m] = __builtin_amdgcn_mfma_f32_16x16x32_bf16(wf[kb % (PF + 1)][j], xf[kb & 1][m], acc[j][m], 0, 0, 0);
    __builtin_amdgcn_sched_barrier(0);
  }
  STAMPW(3, 2);
  const int qd = lane >> 4, sl = lane & 15;
  char* obase = (char*)hh_f + (size_t)(ft * 4 + wave) * 1024 + ((qd >> 1) * 16 + sl) * 16 + (qd & 1) * 8;
#pragma unroll
  for (int fbl = 0; fbl < 2; ++fbl)
#pragma unroll
    for (int m = 0; m < 8; ++m) {
      float o[4];
#pragma unroll
      for (int r = 0; r < 4; ++r) {
        float gt = rbf(acc[fbl * 2][m][r]);
        float up = rbf(acc[fbl * 2 + 1][m][r]);
        gt = gt * (gt > 0.f ? 1.f : 0.f);
        o[r] = gt * up;
      }
      u32x2 pk = {pack2(o[0], o[1]), pack2(o[2], o[3])};
      *(u32x2*)(obase + (size_t)(rb * 8 + m) * 56 * 1024 + fbl * 512) = pk;
    }
  STAMPW(3, 3);
}

__global__ __launch_bounds__(256) void k3_gemm2(const u16* __restrict__ hh_f, const u16* __restrict__ Wd_f,
                                                const u16* __restrict__ hin, u16* __restrict__ out) {
  __shared__ __attribute__((aligned(16))) char red[4 * 16 * 1024];
  const int tid = threadIdx.x;
  const int wave = tid >> 6, lane = tid & 63;
  const int bid = blockIdx.x;
  const int xcd = bid & 7, idx = bid >> 3;
  const int dt = idx & 7, st = xcd * 4 + (idx >> 3);

  STAMPW(4, 0);
  const char* wb = (const char*)Wd_f + (size_t)(dt * 4 * 56 + wave * 14) * 1024 + lane * 16;
  const char* xb = (const char*)hh_f + (size_t)(st * 4 * 56 + wave * 14) * 1024 + lane * 16;
  constexpr int PF = 5;
  bf16x8 wf[PF + 1][4], xf[PF + 1][4];
#pragma unroll
  for (int p = 0; p < PF; ++p)
#pragma unroll
    for (int j = 0; j < 4; ++j) {
      wf[p][j] = *(const bf16x8*)(wb + (j * 56 + p) * 1024);
      xf[p][j] = *(const bf16x8*)(xb + (j * 56 + p) * 1024);
    }
  const int qd = lane >> 4, sl = lane & 15;
  u32x2 hv[4];
#pragma unroll
  for (int j = 0; j < 4; ++j)
    hv[j] = *(const u32x2*)(hin + (size_t)(st * 64 + j * 16 + sl) * D_ + dt * 64 + wave * 16 + qd * 4);
  f32x4 acc[4][4];
#pragma unroll
  for (int i = 0; i < 4; ++i)
#pragma unroll
    for (int j = 0; j < 4; ++j) acc[i][j] = f32x4{0.f, 0.f, 0.f, 0.f};
#pragma unroll
  for (int kb = 0; kb < 14; ++kb) {
    if (kb + PF < 14) {
#pragma unroll
      for (int j = 0; j < 4; ++j) {
        wf[(kb + PF) % (PF + 1)][j] = *(const bf16x8*)(wb + (j * 56 + kb + PF) * 1024);
        xf[(kb + PF) % (PF + 1)][j] = *(const bf16x8*)(xb + (j * 56 + kb + PF) * 1024);
      }
    }
    __builtin_amdgcn_sched_barrier(0);
#pragma unroll
    for (int i = 0; i < 4; ++i)
#pragma unroll
      for (int j = 0; j < 4; ++j)
        acc[i][j] = __builtin_amdgcn_mfma_f32_16x16x32_bf16(wf[kb % (PF + 1)][i], xf[kb % (PF + 1)][j], acc[i][j], 0, 0, 0);
    __builtin_amdgcn_sched_barrier(0);
  }
  STAMPW(4, 1);
#pragma unroll
  for (int i = 0; i < 4; ++i)
#pragma unroll
    for (int j = 0; j < 4; ++j)
      *(f32x4*)(red + ((wave * 4 + i) * 4 + j) * 1024 + lane * 16) = acc[i][j];
  __syncthreads();
#pragma unroll
  for (int j = 0; j < 4; ++j) {
    f32x4 sum = f32x4{0.f, 0.f, 0.f, 0.f};
#pragma unroll
    for (int w = 0; w < 4; ++w) sum += *(const f32x4*)(red + ((w * 4 + wave) * 4 + j) * 1024 + lane * 16);
    float hx[4] = {bflo(hv[j][0]), bfhi(hv[j][0]), bflo(hv[j][1]), bfhi(hv[j][1])};
    float o[4];
#pragma unroll
    for (int r = 0; r < 4; ++r) o[r] = hx[r] + rbf(sum[r]);
    u32x2 pk = {pack2(o[0], o[1]), pack2(o[2], o[3])};
    *(u32x2*)(out + (size_t)(st * 64 + j * 16 + sl) * D_ + dt * 64 + wave * 16 + qd * 4) = pk;
  }
  STAMPW(4, 2);
}

extern "C" void kernel_launch(void* const* d_in, const int* in_sizes, int n_in, void* d_out, int out_size,
                              void* d_ws, size_t ws_size, hipStream_t stream) {
  const u16* q = (const u16*)d_in[0];
  const u16* k = (const u16*)d_in[1];
  const u16* v = (const u16*)d_in[2];
  const u16* shiftk = (const u16*)d_in[3];
  const u16* ssmk = (const u16*)d_in[4];
  const u16* ssmD = (const u16*)d_in[5];
  const u16* Wg = (const u16*)d_in[7];
  const u16* Wu = (const u16*)d_in[8];
  const u16* Wd = (const u16*)d_in[9];
  char* ws = (char*)d_ws;
  u16* y2T = (u16*)(ws);
  u16* hbuf = (u16*)(ws + (2u << 20));
  u16* hln_f = (u16*)(ws + (4u << 20));
  u16* Wgu_f = (u16*)(ws + (6u << 20));
  u16* Wd_f = (u16*)(ws + (10u << 20));
  u16* hh_f = (u16*)(ws + (12u << 20));
  u16* kvT = (u16*)(ws + (20u << 20));

  (void)hipFuncSetAttribute((const void*)k1_conv, hipFuncAttributeMaxDynamicSharedMemorySize, K1_LDS);
  (void)hipFuncSetAttribute((const void*)k2_gemm1, hipFuncAttributeMaxDynamicSharedMemorySize, 131072);

  k0_pre<<<256, 256, 0, stream>>>(k, v, shiftk, Wg, Wu, Wd, kvT, Wgu_f, Wd_f);
  k1_conv<<<256, 256, K1_LDS, stream>>>(kvT, ssmk, ssmD, y2T);
  k1b_rows<<<256, 256, 0, stream>>>(q, y2T, hbuf, hln_f);
  k2_gemm1<<<224, 256, 131072, stream>>>(hln_f, Wgu_f, hh_f);
  k3_gemm2<<<256, 256, 0, stream>>>(hh_f, Wd_f, hbuf, (u16*)d_out);
}
```

```cpp
#include <hip/hip_runtime.h>
#include <hip/hip_bf16.h>

typedef unsigned short u16;
typedef unsigned int u32;
using bf16x8 = __attribute__((ext_vector_type(8))) __bf16;
using f32x4 = __attribute__((ext_vector_type(4))) float;
using f32x16 = __attribute__((ext_vector_type(16))) float;
using u32x4 = __attribute__((ext_vector_type(4))) u32;
using u32x2 = __attribute__((ext_vector_type(2))) u32;

constexpr int S_ = 2048, D_ = 512, F_ = 1792;

#define STAMPW(kid, idx)

__device__ __forceinline__ float bf2f(u32 u) { return __uint_as_float(u << 16); }
__device__ __forceinline__ float bflo(u32 u) { return __uint_as_float(u << 16); }
__device__ __forceinline__ float bfhi(u32 u) { return __uint_as_float(u & 0xffff0000u); }
__device__ __forceinline__ u32 f2bf(float f) {
  __bf16 b = (__bf16)f;
  return (u32)__builtin_bit_cast(u16, b);
}
__device__ __forceinline__ float rbf(float f) { return bf2f(f2bf(f)); }
__device__ __forceinline__ u32 pack2(float lo, float hi) { return f2bf(lo) | (f2bf(hi) << 16); }

__global__ __launch_bounds__(256) void k0_pre(const u16* __restrict__ kin, const u16* __restrict__ vin,
                                              const u16* __restrict__ shiftk, const u16* __restrict__ Wg,
                                              const u16* __restrict__ Wu, const u16* __restrict__ Wd,
                                              u16* __restrict__ kvT, u16* __restrict__ Wgu_f,
                                              u16* __restrict__ Wd_f) {
  const int tid = threadIdx.x;
  const int bid = blockIdx.x;
  STAMPW(0, 0);
  const int ct = bid & 7, rt = bid >> 3;
  const int d0 = ct * 64 + (tid & 31) * 2;
  const int r0 = rt * 64 + (tid >> 5) * 8;
  u32 kw[11], vw[8];
  {
    const u32* kp = (const u32*)(kin + d0);
    const u32* vp = (const u32*)(vin + d0);
#pragma unroll
    for (int i = 0; i < 11; ++i) {
      int r = r0 - 3 + i;
      kw[i] = (r >= 0) ? kp[(size_t)r * (D_ / 2)] : 0u;
    }
#pragma unroll
    for (int i = 0; i < 8; ++i) vw[i] = vp[(size_t)(r0 + i) * (D_ / 2)];
  }
  const u32x2 sw0 = *(const u32x2*)(shiftk + (size_t)d0 * 4);
  const u32x2 sw1 = *(const u32x2*)(shiftk + (size_t)(d0 + 1) * 4);
  if (tid < 168) {
    int blk = bid * 168 + tid;
    int which = blk / 14336;
    int rem = blk - which * 14336;
    const u16* src;
    int ld, kq, nq;
    if (which < 2) {
      kq = rem / 224; nq = rem - kq * 224; ld = F_;
      src = (which == 0 ? Wg : Wu);
    } else {
      kq = rem / 64; nq = rem - kq * 64; ld = D_;
      src = Wd;
    }
    u32x4 rws[8];
#pragma unroll
    for (int i = 0; i < 8; ++i) rws[i] = *(const u32x4*)(src + (size_t)(kq * 8 + i) * ld + nq * 8);
    u32x4 o[8];
#pragma unroll
    for (int n2 = 0; n2 < 4; ++n2) {
#pragma unroll
      for (int kk = 0; kk < 4; ++kk) {
        u32 a = rws[2 * kk][n2], b = rws[2 * kk + 1][n2];
        o[2 * n2][kk] = (a & 0xffffu) | (b << 16);
        o[2 * n2 + 1][kk] = (a >> 16) | (b & 0xffff0000u);
      }
    }
    char* dst;
    if (which < 2) {
      int fb = nq >> 1, kb = kq >> 2;
      dst = (char*)Wgu_f + (size_t)(((fb * 2 + which) * 16 + kb)) * 1024 + ((kq & 3) * 16 + (nq & 1) * 8) * 16;
    } else {
      int db = nq >> 1, kb = kq >> 2;
      dst = (char*)Wd_f + (size_t)(db * 56 + kb) * 1024 + ((kq & 3) * 16 + (nq & 1) * 8) * 16;
    }
#pragma unroll
    for (int n = 0; n < 8; ++n) *(u32x4*)(dst + n * 16) = o[n];
  }
  {
    float w[2][4] = {{bflo(sw0[0]), bfhi(sw0[0]), bflo(sw0[1]), bfhi(sw0[1])},
                     {bflo(sw1[0]), bfhi(sw1[0]), bflo(sw1[1]), bfhi(sw1[1])}};
#pragma unroll
    for (int c = 0; c < 2; ++c) {
      float kf[11];
#pragma unroll
      for (int i = 0; i < 11; ++i) kf[i] = c ? bfhi(kw[i]) : bflo(kw[i]);
      float u[8];
#pragma unroll
      for (int i = 0; i < 8; ++i) {
        float acc = rbf(kf[i] * w[c][3]);
        acc = rbf(acc + rbf(kf[i + 1] * w[c][2]));
        acc = rbf(acc + rbf(kf[i + 2] * w[c][1]));
        acc = rbf(acc + rbf(kf[i + 3] * w[c][0]));
        float vf = c ? bfhi(vw[i]) : bflo(vw[i]);
        u[i] = acc * vf;
      }
      u32x4 pk = {pack2(u[0], u[1]), pack2(u[2], u[3]), pack2(u[4], u[5]), pack2(u[6], u[7])};
      *(u32x4*)(kvT + (size_t)(d0 + c) * S_ + r0) = pk;
    }
  }
  STAMPW(0, 1);
}

constexpr int KCS = 4160;
constexpr int KCH = 8 * KCS;
constexpr int UCH = 64 * 144;
constexpr int K1_KOFF = 0;
constexpr int K1_UOFF = 2 * KCH;
constexpr int K1_ROFF = K1_UOFF + 2 * UCH;
constexpr int K1_LDS = K1_ROFF + 4 * 4096;

__device__ __forceinline__ u32 rot16(u32 x) { return (x >> 16) | (x << 16); }

__global__ __launch_bounds__(256) void k1_conv(const u16* __restrict__ kvT, const u16* __restrict__ ssmk,
                                               const u16* __restrict__ ssmD, u16* __restrict__ y2T) {
  extern __shared__ __attribute__((aligned(16))) char smem[];
  const int tid = threadIdx.x;
  const int d0 = 2 * blockIdx.x;
  STAMPW(1, 0);
  u32x4 g1[2], g0[2], uv[2];
#pragma unroll
  for (int c = 0; c < 2; ++c) {
    const u16* kr = ssmk + (size_t)(d0 + c) * S_;
    int a = 2040 - 8 * tid;
    g1[c] = *(const u32x4*)(kr + a);
    if (a >= 8) g0[c] = *(const u32x4*)(kr + a - 8);
    else g0[c] = u32x4{0, 0, 0, 0};
    uv[c] = *(const u32x4*)(kvT + (size_t)(d0 + c) * S_ + tid * 8);
  }
  const float dval0 = bf2f(ssmD[d0]), dval1 = bf2f(ssmD[d0 + 1]);
  {
    u32x4 z = {0, 0, 0, 0};
    for (int i = tid; i < 2 * 288; i += 256) {
      int c = i / 288, j = i - c * 288;
      *(u32x4*)(smem + K1_UOFF + c * UCH + j * 16) = z;
    }
    if (tid < 64) {
      int c = tid >> 5, r = (tid >> 2) & 7, q = 256 + (tid & 3);
      *(u32x4*)(smem + K1_KOFF + c * KCH + r * KCS + q * 16) = z;
    }
  }
  STAMPW(1, 1);
#pragma unroll
  for (int c = 0; c < 2; ++c) {
    u32 W[8], O[7];
#pragma unroll
    for (int i = 0; i < 4; ++i) {
      W[i] = rot16(g1[c][3 - i]);
      W[4 + i] = rot16(g0[c][3 - i]);
    }
#pragma unroll
    for (int i = 0; i < 7; ++i) O[i] = (W[i] >> 16) | (W[i + 1] << 16);
    char* base = smem + K1_KOFF + c * KCH + tid * 16;
#pragma unroll
    for (int r = 0; r < 8; ++r) {
      u32x4 val;
      if ((r & 1) == 0) val = u32x4{W[r / 2], W[r / 2 + 1], W[r / 2 + 2], W[r / 2 + 3]};
      else val = u32x4{O[r / 2], O[r / 2 + 1], O[r / 2 + 2], O[r / 2 + 3]};
      *(u32x4*)(base + r * KCS) = val;
    }
    *(u32x4*)(smem + K1_UOFF + c * UCH + (32 + (tid >> 3)) * 144 + (tid & 7) * 16) = uv[c];
  }
  __syncthreads();
  STAMPW(1, 2);

  const int wave = tid >> 6, lane = tid & 63;
  const int c = wave >> 1, hf = wave & 1;
  const int b = lane & 31, h = lane >> 5;
  f32x16 acc0, acc1;
#pragma unroll
  for (int i = 0; i < 16; ++i) { acc0[i] = 0.f; acc1[i] = 0.f; }
  {
    const int i0 = 2047 - (-16 + 1024 * hf) - b + 8 * h;
    const int r = i0 & 7;
    const char* ap = smem + K1_KOFF + c * KCH + r * KCS + (i0 - r) * 2 - 32 * 63;
    const char* ub = smem + K1_UOFF + c * UCH + (32 + b - 16 * hf - 16) * 144 + 16 * h;
    auto boff = [](int j) -> int {
      int base = 16 - 16 * j;
      int blkoff = (base >= 0) ? (base >> 6) : -((-base + 63) >> 6);
      int e0 = base - blkoff * 64;
      return (blkoff + 16) * 144 + e0 * 2;
    };
    constexpr int PD = 3;
    bf16x8 af[PD + 1], bfr[PD + 3];
#pragma unroll
    for (int j = -2; j < PD; ++j) bfr[(j + 2) % (PD + 3)] = *(const bf16x8*)(ub + boff(j));
#pragma unroll
    for (int i = 0; i < PD; ++i) af[i % (PD + 1)] = *(const bf16x8*)(ap + 32 * (63 - i));
#pragma unroll
    for (int i = 0; i < 64; ++i) {
      if (i + PD < 64) {
        af[(i + PD) % (PD + 1)] = *(const bf16x8*)(ap + 32 * (63 - i - PD));
        bfr[(i + PD + 2) % (PD + 3)] = *(const bf16x8*)(ub + boff(i + PD));
      }
      acc0 = __builtin_amdgcn_mfma_f32_32x32x16_bf16(af[i % (PD + 1)], bfr[(i + 2) % (PD + 3)], acc0, 0, 0, 0);
      acc1 = __builtin_amdgcn_mfma_f32_32x32x16_bf16(af[i % (PD + 1)], bfr[i % (PD + 3)], acc1, 0, 0, 0);
      __builtin_amdgcn_sched_group_barrier(0x8, 1, 0);
      __builtin_amdgcn_sched_group_barrier(0x100, 1, 0);
      __builtin_amdgcn_sched_group_barrier(0x8, 1, 0);
      __builtin_amdgcn_sched_group_barrier(0x100, 1, 0);
      __builtin_amdgcn_sched_barrier(0);
    }
  }
  STAMPW(1, 3);
  {
    float* mine = (float*)(smem + K1_ROFF + wave * 4096);
    const float* other = (const float*)(smem + K1_ROFF + (wave ^ 1) * 4096);
#pragma unroll
    for (int i = 0; i < 4; ++i) {
      f32x4 x;
#pragma unroll
      for (int j = 0; j < 4; ++j) x[j] = hf ? acc0[4 * i + j] : acc1[4 * i + j];
      *(f32x4*)(mine + (i * 64 + lane) * 4) = x;
    }
    __syncthreads();
    f32x16 acc = hf ? acc1 : acc0;
#pragma unroll
    for (int i = 0; i < 4; ++i) {
      f32x4 x = *(const f32x4*)(other + (i * 64 + lane) * 4);
#pragma unroll
      for (int j = 0; j < 4; ++j) acc[4 * i + j] += x[j];
    }
    const float dval = c ? dval1 : dval0;
    u16* orow = y2T + (size_t)(d0 + c) * 8 + 4 * h;
#pragma unroll
    for (int i = 0; i < 4; ++i) {
      int e = 32 * hf + 8 * i + 4 * h;
      u32x2 kvp = *(const u32x2*)(smem + K1_UOFF + c * UCH + (32 + b) * 144 + e * 2);
      float kv[4] = {bflo(kvp[0]), bfhi(kvp[0]), bflo(kvp[1]), bfhi(kvp[1])};
      float o[4];
#pragma unroll
      for (int j = 0; j < 4; ++j) o[j] = rbf(acc[4 * i + j]) + rbf(kv[j] * dval);
      u32x2 pk = {pack2(o[0], o[1]), pack2(o[2], o[3])};
      *(u32x2*)(orow + (size_t)(8 * b + 4 * hf + i) * (D_ * 8)) = pk;
    }
  }
  STAMPW(1, 4);
}

template <int CTRL>
__device__ __forceinline__ float dpp_add(float x) {
  int y = __builtin_amdgcn_update_dpp(0, __float_as_int(x), CTRL, 0xf, 0xf, true);
  return x + __int_as_float(y);
}
__device__ __forceinline__ float wave_sum(float x) {
  x = dpp_add<0xB1>(x);
  x = dpp_add<0x4E>(x);
  x = dpp_add<0x141>(x);
  x = dpp_add<0x140>(x);
  int xi = __float_as_int(x);
  float a = __int_as_float(__builtin_amdgcn_readlane(xi, 0));
  float b = __int_as_float(__builtin_amdgcn_readlane(xi, 16));
  float c = __int_as_float(__builtin_amdgcn_readlane(xi, 32));
  float d = __int_as_float(__builtin_amdgcn_readlane(xi, 48));
  return (a + b) + (c + d);
}

__device__ __forceinline__ void ln_bf16x2(const float (&x)[2][8], float (&o)[2][8]) {
  float s[2], ss[2], mean[2];
#pragma unroll
  for (int r = 0; r < 2; ++r) {
    s[r] = ((x[r][0] + x[r][1]) + (x[r][2] + x[r][3])) + ((x[r][4] + x[r][5]) + (x[r][6] + x[r][7]));
  }
#pragma unroll
  for (int r = 0; r < 2; ++r) mean[r] = wave_sum(s[r]) * (1.f / 512.f);
#pragma unroll
  for (int r = 0; r < 2; ++r) {
    ss[r] = 0.f;
#pragma unroll
    for (int i = 0; i < 8; ++i) { float dlt = x[r][i] - mean[r]; ss[r] += dlt * dlt; }
  }
#pragma unroll
  for (int r = 0; r < 2; ++r) {
    const float var = wave_sum(ss[r]) * (1.f / 512.f);
    const float mb = rbf(mean[r]);
    const float vb = rbf(var);
    const float sd = rbf(sqrtf(rbf(vb + 1e-5f)));
    const float rc = 1.0f / sd;
#pragma unroll
    for (int i = 0; i < 8; ++i) {
      float a = rbf(x[r][i] - mb);
      float qt = a * rc;
      float rem = __builtin_fmaf(-qt, sd, a);
      qt = __builtin_fmaf(rem, rc, qt);
      o[r][i] = rbf(qt);
    }
  }
}

__global__ __launch_bounds__(256) void k1b_rows(const u16* __restrict__ q, const u16* __restrict__ y2T,
                                                u16* __restrict__ hout, u16* __restrict__ hln_f) {
  __shared__ __attribute__((aligned(16))) u32 tile[8][264];
  const int tid = threadIdx.x;
  const int s0 = blockIdx.x * 8;
  const int wave = tid >> 6, lane = tid & 63;
  STAMPW(2, 0);
  u32x4 ya = *(const u32x4*)(y2T + (size_t)blockIdx.x * (D_ * 8) + (2 * tid) * 8);
  u32x4 yb = *(const u32x4*)(y2T + (size_t)blockIdx.x * (D_ * 8) + (2 * tid + 1) * 8);
  u32x4 qv[2];
#pragma unroll
  for (int r = 0; r < 2; ++r) qv[r] = *(const u32x4*)(q + (size_t)(s0 + 2 * wave + r) * D_ + lane * 8);
  STAMPW(2, 1);
#pragma unroll
  for (int i = 0; i < 4; ++i) {
    tile[2 * i][tid] = (ya[i] & 0xffffu) | (yb[i] << 16);
    tile[2 * i + 1][tid] = (ya[i] >> 16) | (yb[i] & 0xffff0000u);
  }
  float x[2][8], ql[2][8];
#pragma unroll
  for (int r = 0; r < 2; ++r)
#pragma unroll
    for (int i = 0; i < 4; ++i) {
      x[r][2 * i] = bflo(qv[r][i]);
      x[r][2 * i + 1] = bfhi(qv[r][i]);
    }
  ln_bf16x2(x, ql);
  __syncthreads();
  float hh[2][8], hl[2][8];
#pragma unroll
  for (int r = 0; r < 2; ++r) {
    u32x4 yv = *(const u32x4*)(&tile[2 * wave + r][lane * 4]);
#pragma unroll
    for (int i = 0; i < 4; ++i) {
      hh[r][2 * i] = rbf(x[r][2 * i] + rbf(ql[r][2 * i] * bflo(yv[i])));
      hh[r][2 * i + 1] = rbf(x[r][2 * i + 1] + rbf(ql[r][2 * i + 1] * bfhi(yv[i])));
    }
  }
  ln_bf16x2(hh, hl);
#pragma unroll
  for (int r = 0; r < 2; ++r) {
    const int s = s0 + 2 * wave + r;
    u32x4 hp = {pack2(hh[r][0], hh[r][1]), pack2(hh[r][2], hh[r][3]), pack2(hh[r][4], hh[r][5]),
                pack2(hh[r][6], hh[r][7])};
    u32x4 lp = {pack2(hl[r][0], hl[r][1]), pack2(hl[r][2], hl[r][3]), pack2(hl[r][4], hl[r][5]),
                pack2(hl[r][6], hl[r][7])};
    *(u32x4*)(hout + (size_t)s * D_ + lane * 8) = hp;
    char* dst = (char*)hln_f + (size_t)(((s >> 7) * 16 + (lane >> 2)) * 8 + ((s >> 4) & 7)) * 1024 +
                ((lane & 3) * 16 + (s & 15)) * 16;
    *(u32x4*)dst = lp;
  }
  STAMPW(2, 2);
}

#define SGB(mask, n) __builtin_amdgcn_sched_group_barrier(mask, n, 0)
__global__ __launch_bounds__(256) void k2_gemm1(const u16* __restrict__ hln_f, const u16* __restrict__ Wgu_f,
                                                u16* __restrict__ hh_f) {
  extern __shared__ __attribute__((aligned(16))) char smem[];
  const int tid = threadIdx.x;
  const int wave = tid >> 6, lane = tid & 63;
  const int bid = blockIdx.x;
  const int xcd = bid & 7, idx = bid >> 3;
  const int ft = (xcd & 1) * 7 + (idx % 7);
  const int rb = (xcd >> 1) * 4 + (idx / 7);

  STAMPW(3, 0);
  const char* asrc = (const char*)hln_f + (size_t)rb * 131072 + tid * 16;
  u32x4 ast[2][8];
#pragma unroll
  for (int i = 0; i < 8; ++i) ast[0][i] = *(const u32x4*)(asrc + i * 4096);
  const char* wbase = (const char*)Wgu_f + (size_t)((ft * 8 + wave * 2) * 2) * 16 * 1024 + lane * 16;
  constexpr int PF = 4;
  bf16x8 wf[PF + 1][4];
#pragma unroll
  for (int p = 0; p < PF; ++p)
#pragma unroll
    for (int j = 0; j < 4; ++j) wf[p][j] = *(const bf16x8*)(wbase + (j * 16 + p) * 1024);
#pragma unroll
  for (int i = 0; i < 8; ++i) ast[1][i] = *(const u32x4*)(asrc + 32768 + i * 4096);

  f32x4 acc[4][8];
#pragma unroll
  for (int j = 0; j < 4; ++j)
#pragma unroll
    for (int m = 0; m < 8; ++m) acc[j][m] = f32x4{0.f, 0.f, 0.f, 0.f};

  bf16x8 xf[2][8];
#pragma unroll
  for (int ch = 0; ch < 4; ++ch) {
    __builtin_amdgcn_sched_barrier(0);
#pragma unroll
    for (int i = 0; i < 8; ++i) *(u32x4*)(smem + ch * 32768 + i * 4096 + tid * 16) = ast[ch & 1][i];
    if (ch + 2 < 4) {
#pragma unroll
      for (int i = 0; i < 8; ++i) ast[ch & 1][i] = *(const u32x4*)(asrc + (ch + 2) * 32768 + i * 4096);
    }
    __syncthreads();
    if (ch == 0) STAMPW(3, 1);
#pragma unroll
    for (int m = 0; m < 8; ++m) xf[0][m] = *(const bf16x8*)(smem + (ch * 32 + m) * 1024 + lane * 16);
    __builtin_amdgcn_sched_barrier(0);
#pragma unroll
    for (int k4 = 0; k4 < 4; ++k4) {
      const int kb = ch * 4 + k4;
      if (kb + PF < 16) {
#pragma unroll
        for (int j = 0; j < 4; ++j)
          wf[(kb + PF) % (PF + 1)][j] = *(const bf16x8*)(wbase + (j * 16 + kb + PF) * 1024);
      }
      if (k4 + 1 < 4) {
#pragma unroll
        for (int m = 0; m < 8; ++m)
          xf[(k4 + 1) & 1][m] = *(const bf16x8*)(smem + ((kb + 1) * 8 + m) * 1024 + lane * 16);
      }
#pragma unroll
      for (int j = 0; j < 4; ++j)
#pragma unroll
        for (int m = 0; m < 8; ++m)
          acc[j][m] = __builtin_amdgcn_mfma_f32_16x16x32_bf16(wf[kb % (PF + 1)][j], xf[k4 & 1][m], acc[j][m], 0, 0, 0);
#pragma unroll
      for (int g = 0; g < 4; ++g) {
        SGB(0x8, 4); SGB(0x100, 1); SGB(0x8, 4); SGB(0x100, 1); SGB(0x20, 1);
      }
      __builtin_amdgcn_sched_barrier(0);
    }
  }
  STAMPW(3, 2);
  const int qd = lane >> 4, sl = lane & 15;
  char* obase = (char*)hh_f + (size_t)(ft * 4 + wave) * 1024 + ((qd >> 1) * 16 + sl) * 16 + (qd & 1) * 8;
#pragma unroll
  for (int fbl = 0; fbl < 2; ++fbl)
#pragma unroll
    for (int m = 0; m < 8; ++m) {
      float o[4];
#pragma unroll
      for (int r = 0; r < 4; ++r) {
        float gt = rbf(acc[fbl * 2][m][r]);
        float up = rbf(acc[fbl * 2 + 1][m][r]);
        gt = gt * (gt > 0.f ? 1.f : 0.f);
        o[r] = gt * up;
      }
      u32x2 pk = {pack2(o[0], o[1]), pack2(o[2], o[3])};
      *(u32x2*)(obase + (size_t)(rb * 8 + m) * 56 * 1024 + fbl * 512) = pk;
    }
  STAMPW(3, 3);
}

__global__ __launch_bounds__(256) void k3_gemm2(const u16* __restrict__ hh_f, const u16* __restrict__ Wd_f,
                                                const u16* __restrict__ hin, u16* __restrict__ out) {
  __shared__ __attribute__((aligned(16))) char red[4 * 16 * 1024];
  const int tid = threadIdx.x;
  const int wave = tid >> 6, lane = tid & 63;
  const int bid = blockIdx.x;
  const int xcd = bid & 7, idx = bid >> 3;
  const int dt = idx & 7, st = xcd * 4 + (idx >> 3);

  STAMPW(4, 0);
  const char* wb = (const char*)Wd_f + (size_t)(dt * 4 * 56 + wave * 14) * 1024 + lane * 16;
  const char* xb = (const char*)hh_f + (size_t)(st * 4 * 56 + wave * 14) * 1024 + lane * 16;
  constexpr int PF = 5;
  bf16x8 wf[PF + 1][4], xf[PF + 1][4];
#pragma unroll
  for (int p = 0; p < PF; ++p)
#pragma unroll
    for (int j = 0; j < 4; ++j) {
      wf[p][j] = *(const bf16x8*)(wb + (j * 56 + p) * 1024);
      xf[p][j] = *(const bf16x8*)(xb + (j * 56 + p) * 1024);
    }
  const int qd = lane >> 4, sl = lane & 15;
  u32x2 hv[4];
#pragma unroll
  for (int j = 0; j < 4; ++j)
    hv[j] = *(const u32x2*)(hin + (size_t)(st * 64 + j * 16 + sl) * D_ + dt * 64 + wave * 16 + qd * 4);
  f32x4 acc[4][4];
#pragma unroll
  for (int i = 0; i < 4; ++i)
#pragma unroll
    for (int j = 0; j < 4; ++j) acc[i][j] = f32x4{0.f, 0.f, 0.f, 0.f};
  __builtin_amdgcn_sched_barrier(0);
#pragma unroll
  for (int kb = 0; kb < 14; ++kb) {
    if (kb + PF < 14) {
#pragma unroll
      for (int j = 0; j < 4; ++j) {
        wf[(kb + PF) % (PF + 1)][j] = *(const bf16x8*)(wb + (j * 56 + kb + PF) * 1024);
        xf[(kb + PF) % (PF + 1)][j] = *(const bf16x8*)(xb + (j * 56 + kb + PF) * 1024);
      }
    }
#pragma unroll
    for (int i = 0; i < 4; ++i)
#pragma unroll
      for (int j = 0; j < 4; ++j)
        acc[i][j] = __builtin_amdgcn_mfma_f32_16x16x32_bf16(wf[kb % (PF + 1)][i], xf[kb % (PF + 1)][j], acc[i][j], 0, 0, 0);
#pragma unroll
    for (int g = 0; g < 8; ++g) { SGB(0x8, 2); SGB(0x20, 1); }
    __builtin_amdgcn_sched_barrier(0);
  }
  STAMPW(4, 1);
#pragma unroll
  for (int i = 0; i < 4; ++i)
#pragma unroll
    for (int j = 0; j < 4; ++j)
      *(f32x4*)(red + ((wave * 4 + i) * 4 + j) * 1024 + lane * 16) = acc[i][j];
  __syncthreads();
#pragma unroll
  for (int j = 0; j < 4; ++j) {
    f32x4 sum = f32x4{0.f, 0.f, 0.f, 0.f};
#pragma unroll
    for (int w = 0; w < 4; ++w) sum += *(const f32x4*)(red + ((w * 4 + wave) * 4 + j) * 1024 + lane * 16);
    float hx[4] = {bflo(hv[j][0]), bfhi(hv[j][0]), bflo(hv[j][1]), bfhi(hv[j][1])};
    float o[4];
#pragma unroll
    for (int r = 0; r < 4; ++r) o[r] = hx[r] + rbf(sum[r]);
    u32x2 pk = {pack2(o[0], o[1]), pack2(o[2], o[3])};
    *(u32x2*)(out + (size_t)(st * 64 + j * 16 + sl) * D_ + dt * 64 + wave * 16 + qd * 4) = pk;
  }
  STAMPW(4, 2);
}

extern "C" void kernel_launch(void* const* d_in, const int* in_sizes, int n_in, void* d_out, int out_size,
                              void* d_ws, size_t ws_size, hipStream_t stream) {
  const u16* q = (const u16*)d_in[0];
  const u16* k = (const u16*)d_in[1];
  const u16* v = (const u16*)d_in[2];
  const u16* shiftk = (const u16*)d_in[3];
  const u16* ssmk = (const u16*)d_in[4];
  const u16* ssmD = (const u16*)d_in[5];
  const u16* Wg = (const u16*)d_in[7];
  const u16* Wu = (const u16*)d_in[8];
  const u16* Wd = (const u16*)d_in[9];
  char* ws = (char*)d_ws;
  u16* y2T = (u16*)(ws);
  u16* hbuf = (u16*)(ws + (2u << 20));
  u16* hln_f = (u16*)(ws + (4u << 20));
  u16* Wgu_f = (u16*)(ws + (6u << 20));
  u16* Wd_f = (u16*)(ws + (10u << 20));
  u16* hh_f = (u16*)(ws + (12u << 20));
  u16* kvT = (u16*)(ws + (20u << 20));

  (void)hipFuncSetAttribute((const void*)k1_conv, hipFuncAttributeMaxDynamicSharedMemorySize, K1_LDS);
  (void)hipFuncSetAttribute((const void*)k2_gemm1, hipFuncAttributeMaxDynamicSharedMemorySize, 131072);

  k0_pre<<<256, 256, 0, stream>>>(k, v, shiftk, Wg, Wu, Wd, kvT, Wgu_f, Wd_f);
  k1_conv<<<256, 256, K1_LDS, stream>>>(kvT, ssmk, ssmD, y2T);
  k1b_rows<<<256, 256, 0, stream>>>(q, y2T, hbuf, hln_f);
  k2_gemm1<<<224, 256, 131072, stream>>>(hln_f, Wgu_f, hh_f);
  k3_gemm2<<<256, 256, 0, stream>>>(hh_f, Wd_f, hbuf, (u16*)d_out);
}
```

```cpp
#include <hip/hip_runtime.h>
#include <hip/hip_bf16.h>

typedef unsigned short u16;
typedef unsigned int u32;
using bf16x8 = __attribute__((ext_vector_type(8))) __bf16;
using f32x4 = __attribute__((ext_vector_type(4))) float;
using f32x16 = __attribute__((ext_vector_type(16))) float;
using u32x4 = __attribute__((ext_vector_type(4))) u32;
using u32x2 = __attribute__((ext_vector_type(2))) u32;

constexpr int S_ = 2048, D_ = 512, F_ = 1792;

#define STAMPW(kid, idx)

__device__ __forceinline__ float bf2f(u32 u) { return __uint_as_float(u << 16); }
__device__ __forceinline__ float bflo(u32 u) { return __uint_as_float(u << 16); }
__device__ __forceinline__ float bfhi(u32 u) { return __uint_as_float(u & 0xffff0000u); }
__device__ __forceinline__ u32 f2bf(float f) {
  __bf16 b = (__bf16)f;
  return (u32)__builtin_bit_cast(u16, b);
}
__device__ __forceinline__ float rbf(float f) { return bf2f(f2bf(f)); }
__device__ __forceinline__ u32 pack2(float lo, float hi) { return f2bf(lo) | (f2bf(hi) << 16); }

__global__ __launch_bounds__(256) void k0_pre(const u16* __restrict__ kin, const u16* __restrict__ vin,
                                              const u16* __restrict__ shiftk, u16* __restrict__ kvT) {
  const int tid = threadIdx.x;
  const int bid = blockIdx.x;
  STAMPW(0, 0);
  const int ct = bid & 7, rt = bid >> 3;
  const int d0 = ct * 64 + (tid & 31) * 2;
  const int r0 = rt * 64 + (tid >> 5) * 8;
  u32 kw[11], vw[8];
  {
    const u32* kp = (const u32*)(kin + d0);
    const u32* vp = (const u32*)(vin + d0);
#pragma unroll
    for (int i = 0; i < 11; ++i) {
      int r = r0 - 3 + i;
      kw[i] = (r >= 0) ? kp[(size_t)r * (D_ / 2)] : 0u;
    }
#pragma unroll
    for (int i = 0; i < 8; ++i) vw[i] = vp[(size_t)(r0 + i) * (D_ / 2)];
  }
  const u32x2 sw0 = *(const u32x2*)(shiftk + (size_t)d0 * 4);
  const u32x2 sw1 = *(const u32x2*)(shiftk + (size_t)(d0 + 1) * 4);
  {
    float w[2][4] = {{bflo(sw0[0]), bfhi(sw0[0]), bflo(sw0[1]), bfhi(sw0[1])},
                     {bflo(sw1[0]), bfhi(sw1[0]), bflo(sw1[1]), bfhi(sw1[1])}};
#pragma unroll
    for (int c = 0; c < 2; ++c) {
      float kf[11];
#pragma unroll
      for (int i = 0; i < 11; ++i) kf[i] = c ? bfhi(kw[i]) : bflo(kw[i]);
      float u[8];
#pragma unroll
      for (int i = 0; i < 8; ++i) {
        float acc = rbf(kf[i] * w[c][3]);
        acc = rbf(acc + rbf(kf[i + 1] * w[c][2]));
        acc = rbf(acc + rbf(kf[i + 2] * w[c][1]));
        acc = rbf(acc + rbf(kf[i + 3] * w[c][0]));
        float vf = c ? bfhi(vw[i]) : bflo(vw[i]);
        u[i] = acc * vf;
      }
      u32x4 pk = {pack2(u[0], u[1]), pack2(u[2], u[3]), pack2(u[4], u[5]), pack2(u[6], u[7])};
      *(u32x4*)(kvT + (size_t)(d0 + c) * S_ + r0) = pk;
    }
  }
  STAMPW(0, 1);
}

constexpr int KCS = 4160;
constexpr int KCH = 8 * KCS;
constexpr int UCH = 64 * 144;
constexpr int K1_KOFF = 0;
constexpr int K1_UOFF = 2 * KCH;
constexpr int K1_ROFF = K1_UOFF + 2 * UCH;
constexpr int K1_LDS = K1_ROFF + 4 * 4096;

__device__ __forceinline__ u32 rot16(u32 x) { return (x >> 16) | (x << 16); }

__global__ __launch_bounds__(256) void k1_conv(const u16* __restrict__ kvT, const u16* __restrict__ ssmk,
                                               const u16* __restrict__ ssmD, u16* __restrict__ y2T,
                                               const u16* __restrict__ Wg, const u16* __restrict__ Wu,
                                               const u16* __restrict__ Wd, u16* __restrict__ Wgu_f,
                                               u16* __restrict__ Wd_f) {
  extern __shared__ __attribute__((aligned(16))) char smem[];
  const int tid = threadIdx.x;
  const int d0 = 2 * blockIdx.x;
  const int bid = blockIdx.x;
  STAMPW(1, 0);
  u32x4 g1[2], g0[2], uv[2];
#pragma unroll
  for (int c = 0; c < 2; ++c) {
    const u16* kr = ssmk + (size_t)(d0 + c) * S_;
    int a = 2040 - 8 * tid;
    g1[c] = *(const u32x4*)(kr + a);
    if (a >= 8) g0[c] = *(const u32x4*)(kr + a - 8);
    else g0[c] = u32x4{0, 0, 0, 0};
    uv[c] = *(const u32x4*)(kvT + (size_t)(d0 + c) * S_ + tid * 8);
  }
  const float dval0 = bf2f(ssmD[d0]), dval1 = bf2f(ssmD[d0 + 1]);
  u32x4 rws[8];
  char* wdst = nullptr;
  if (tid < 168) {
    int blk = bid * 168 + tid;
    int which = blk / 14336;
    int rem = blk - which * 14336;
    const u16* src;
    int ld, kq, nq;
    if (which < 2) {
      kq = rem / 224; nq = rem - kq * 224; ld = F_;
      src = (which == 0 ? Wg : Wu);
    } else {
      kq = rem / 64; nq = rem - kq * 64; ld = D_;
      src = Wd;
    }
#pragma unroll
    for (int i = 0; i < 8; ++i) rws[i] = *(const u32x4*)(src + (size_t)(kq * 8 + i) * ld + nq * 8);
    if (which < 2) {
      int fb = nq >> 1, kb = kq >> 2;
      wdst = (char*)Wgu_f + (size_t)(((fb * 2 + which) * 16 + kb)) * 1024 + ((kq & 3) * 16 + (nq & 1) * 8) * 16;
    } else {
      int db = nq >> 1, kb = kq >> 2;
      wdst = (char*)Wd_f + (size_t)(db * 56 + kb) * 1024 + ((kq & 3) * 16 + (nq & 1) * 8) * 16;
    }
  }
  {
    u32x4 z = {0, 0, 0, 0};
    for (int i = tid; i < 2 * 288; i += 256) {
      int c = i / 288, j = i - c * 288;
      *(u32x4*)(smem + K1_UOFF + c * UCH + j * 16) = z;
    }
    if (tid < 64) {
      int c = tid >> 5, r = (tid >> 2) & 7, q = 256 + (tid & 3);
      *(u32x4*)(smem + K1_KOFF + c * KCH + r * KCS + q * 16) = z;
    }
  }
  STAMPW(1, 1);
#pragma unroll
  for (int c = 0; c < 2; ++c) {
    u32 W[8], O[7];
#pragma unroll
    for (int i = 0; i < 4; ++i) {
      W[i] = rot16(g1[c][3 - i]);
      W[4 + i] = rot16(g0[c][3 - i]);
    }
#pragma unroll
    for (int i = 0; i < 7; ++i) O[i] = (W[i] >> 16) | (W[i + 1] << 16);
    char* base = smem + K1_KOFF + c * KCH + tid * 16;
#pragma unroll
    for (int r = 0; r < 8; ++r) {
      u32x4 val;
      if ((r & 1) == 0) val = u32x4{W[r / 2], W[r / 2 + 1], W[r / 2 + 2], W[r / 2 + 3]};
      else val = u32x4{O[r / 2], O[r / 2 + 1], O[r / 2 + 2], O[r / 2 + 3]};
      *(u32x4*)(base + r * KCS) = val;
    }
    *(u32x4*)(smem + K1_UOFF + c * UCH + (32 + (tid >> 3)) * 144 + (tid & 7) * 16) = uv[c];
  }
  __syncthreads();
  if (tid < 168) {
    u32x4 o[8];
#pragma unroll
    for (int n2 = 0; n2 < 4; ++n2) {
#pragma unroll
      for (int kk = 0; kk < 4; ++kk) {
        u32 a = rws[2 * kk][n2], b = rws[2 * kk + 1][n2];
        o[2 * n2][kk] = (a & 0xffffu) | (b << 16);
        o[2 * n2 + 1][kk] = (a >> 16) | (b & 0xffff0000u);
      }
    }
#pragma unroll
    for (int n = 0; n < 8; ++n) *(u32x4*)(wdst + n * 16) = o[n];
  }
  STAMPW(1, 2);

  const int wave = tid >> 6, lane = tid & 63;
  const int c = wave >> 1, hf = wave & 1;
  const int b = lane & 31, h = lane >> 5;
  f32x16 acc0, acc1;
#pragma unroll
  for (int i = 0; i < 16; ++i) { acc0[i] = 0.f; acc1[i] = 0.f; }
  {
    const int i0 = 2047 - (-16 + 1024 * hf) - b + 8 * h;
    const int r = i0 & 7;
    const char* ap = smem + K1_KOFF + c * KCH + r * KCS + (i0 - r) * 2 - 32 * 63;
    const char* ub = smem + K1_UOFF + c * UCH + (32 + b - 16 * hf - 16) * 144 + 16 * h;
    auto boff = [](int j) -> int {
      int base = 16 - 16 * j;
      int blkoff = (base >= 0) ? (base >> 6) : -((-base + 63) >> 6);
      int e0 = base - blkoff * 64;
      return (blkoff + 16) * 144 + e0 * 2;
    };
    constexpr int PD = 3;
    bf16x8 af[PD + 1], bfr[PD + 3];
#pragma unroll
    for (int j = -2; j < PD; ++j) bfr[(j + 2) % (PD + 3)] = *(const bf16x8*)(ub + boff(j));
#pragma unroll
    for (int i = 0; i < PD; ++i) af[i % (PD + 1)] = *(const bf16x8*)(ap + 32 * (63 - i));
#pragma unroll
    for (int i = 0; i < 64; ++i) {
      if (i + PD < 64) {
        af[(i + PD) % (PD + 1)] = *(const bf16x8*)(ap + 32 * (63 - i - PD));
        bfr[(i + PD + 2) % (PD + 3)] = *(const bf16x8*)(ub + boff(i + PD));
      }
      acc0 = __builtin_amdgcn_mfma_f32_32x32x16_bf16(af[i % (PD + 1)], bfr[(i + 2) % (PD + 3)], acc0, 0, 0, 0);
      acc1 = __builtin_amdgcn_mfma_f32_32x32x16_bf16(af[i % (PD + 1)], bfr[i % (PD + 3)], acc1, 0, 0, 0);
      __builtin_amdgcn_sched_group_barrier(0x8, 1, 0);
      __builtin_amdgcn_sched_group_barrier(0x100, 1, 0);
      __builtin_amdgcn_sched_group_barrier(0x8, 1, 0);
      __builtin_amdgcn_sched_group_barrier(0x100, 1, 0);
      __builtin_amdgcn_sched_barrier(0);
    }
  }
  STAMPW(1, 3);
  {
    float* mine = (float*)(smem + K1_ROFF + wave * 4096);
    const float* other = (const float*)(smem + K1_ROFF + (wave ^ 1) * 4096);
#pragma unroll
    for (int i = 0; i < 4; ++i) {
      f32x4 x;
#pragma unroll
      for (int j = 0; j < 4; ++j) x[j] = hf ? acc0[4 * i + j] : acc1[4 * i + j];
      *(f32x4*)(mine + (i * 64 + lane) * 4) = x;
    }
    __syncthreads();
    f32x16 acc = hf ? acc1 : acc0;
#pragma unroll
    for (int i = 0; i < 4; ++i) {
      f32x4 x = *(const f32x4*)(other + (i * 64 + lane) * 4);
#pragma unroll
      for (int j = 0; j < 4; ++j) acc[4 * i + j] += x[j];
    }
    const float dval = c ? dval1 : dval0;
    u16* orow = y2T + (size_t)(d0 + c) * 8 + 4 * h;
#pragma unroll
    for (int i = 0; i < 4; ++i) {
      int e = 32 * hf + 8 * i + 4 * h;
      u32x2 kvp = *(const u32x2*)(smem + K1_UOFF + c * UCH + (32 + b) * 144 + e * 2);
      float kv[4] = {bflo(kvp[0]), bfhi(kvp[0]), bflo(kvp[1]), bfhi(kvp[1])};
      float o[4];
#pragma unroll
      for (int j = 0; j < 4; ++j) o[j] = rbf(acc[4 * i + j]) + rbf(kv[j] * dval);
      u32x2 pk = {pack2(o[0], o[1]), pack2(o[2], o[3])};
      *(u32x2*)(orow + (size_t)(8 * b + 4 * hf + i) * (D_ * 8)) = pk;
    }
  }
  STAMPW(1, 4);
}

template <int CTRL>
__device__ __forceinline__ float dpp_add(float x) {
  int y = __builtin_amdgcn_update_dpp(0, __float_as_int(x), CTRL, 0xf, 0xf, true);
  return x + __int_as_float(y);
}
__device__ __forceinline__ float wave_sum(float x) {
  x = dpp_add<0xB1>(x);
  x = dpp_add<0x4E>(x);
  x = dpp_add<0x141>(x);
  x = dpp_add<0x140>(x);
  int xi = __float_as_int(x);
  float a = __int_as_float(__builtin_amdgcn_readlane(xi, 0));
  float b = __int_as_float(__builtin_amdgcn_readlane(xi, 16));
  float c = __int_as_float(__builtin_amdgcn_readlane(xi, 32));
  float d = __int_as_float(__builtin_amdgcn_readlane(xi, 48));
  return (a + b) + (c + d);
}

__device__ __forceinline__ void ln_bf16x2(const float (&x)[2][8], float (&o)[2][8]) {
  float s[2], ss[2], mean[2];
#pragma unroll
  for (int r = 0; r < 2; ++r) {
    s[r] = ((x[r][0] + x[r][1]) + (x[r][2] + x[r][3])) + ((x[r][4] + x[r][5]) + (x[r][6] + x[r][7]));
  }
#pragma unroll
  for (int r = 0; r < 2; ++r) mean[r] = wave_sum(s[r]) * (1.f / 512.f);
#pragma unroll
  for (int r = 0; r < 2; ++r) {
    ss[r] = 0.f;
#pragma unroll
    for (int i = 0; i < 8; ++i) { float dlt = x[r][i] - mean[r]; ss[r] += dlt * dlt; }
  }
#pragma unroll
  for (int r = 0; r < 2; ++r) {
    const float var = wave_sum(ss[r]) * (1.f / 512.f);
    const float mb = rbf(mean[r]);
    const float vb = rbf(var);
    const float sd = rbf(sqrtf(rbf(vb + 1e-5f)));
    const float rc = 1.0f / sd;
#pragma unroll
    for (int i = 0; i < 8; ++i) {
      float a = rbf(x[r][i] - mb);
      float qt = a * rc;
      float rem = __builtin_fmaf(-qt, sd, a);
      qt = __builtin_fmaf(rem, rc, qt);
      o[r][i] = rbf(qt);
    }
  }
}

__global__ __launch_bounds__(256) void k1b_rows(const u16* __restrict__ q, const u16* __restrict__ y2T,
                                                u16* __restrict__ hout, u16* __restrict__ hln_f) {
  __shared__ __attribute__((aligned(16))) u32 tile[8][264];
  const int tid = threadIdx.x;
  const int s0 = blockIdx.x * 8;
  const int wave = tid >> 6, lane = tid & 63;
  STAMPW(2, 0);
  u32x4 ya = *(const u32x4*)(y2T + (size_t)blockIdx.x * (D_ * 8) + (2 * tid) * 8);
  u32x4 yb = *(const u32x4*)(y2T + (size_t)blockIdx.x * (D_ * 8) + (2 * tid + 1) * 8);
  u32x4 qv[2];
#pragma unroll
  for (int r = 0; r < 2; ++r) qv[r] = *(const u32x4*)(q + (size_t)(s0 + 2 * wave + r) * D_ + lane * 8);
  STAMPW(2, 1);
#pragma unroll
  for (int i = 0; i < 4; ++i) {
    tile[2 * i][tid] = (ya[i] & 0xffffu) | (yb[i] << 16);
    tile[2 * i + 1][tid] = (ya[i] >> 16) | (yb[i] & 0xffff0000u);
  }
  float x[2][8], ql[2][8];
#pragma unroll
  for (int r = 0; r < 2; ++r)
#pragma unroll
    for (int i = 0; i < 4; ++i) {
      x[r][2 * i] = bflo(qv[r][i]);
      x[r][2 * i + 1] = bfhi(qv[r][i]);
    }
  ln_bf16x2(x, ql);
  __syncthreads();
  float hh[2][8], hl[2][8];
#pragma unroll
  for (int r = 0; r < 2; ++r) {
    u32x4 yv = *(const u32x4*)(&tile[2 * wave + r][lane * 4]);
#pragma unroll
    for (int i = 0; i < 4; ++i) {
      hh[r][2 * i] = rbf(x[r][2 * i] + rbf(ql[r][2 * i] * bflo(yv[i])));
      hh[r][2 * i + 1] = rbf(x[r][2 * i + 1] + rbf(ql[r][2 * i + 1] * bfhi(yv[i])));
    }
  }
  ln_bf16x2(hh, hl);
#pragma unroll
  for (int r = 0; r < 2; ++r) {
    const int s = s0 + 2 * wave + r;
    u32x4 hp = {pack2(hh[r][0], hh[r][1]), pack2(hh[r][2], hh[r][3]), pack2(hh[r][4], hh[r][5]),
                pack2(hh[r][6], hh[r][7])};
    u32x4 lp = {pack2(hl[r][0], hl[r][1]), pack2(hl[r][2], hl[r][3]), pack2(hl[r][4], hl[r][5]),
                pack2(hl[r][6], hl[r][7])};
    *(u32x4*)(hout + (size_t)s * D_ + lane * 8) = hp;
    char* dst = (char*)hln_f + (size_t)(((s >> 7) * 16 + (lane >> 2)) * 8 + ((s >> 4) & 7)) * 1024 +
                ((lane & 3) * 16 + (s & 15)) * 16;
    *(u32x4*)dst = lp;
  }
  STAMPW(2, 2);
}

#define SGB(mask, n) __builtin_amdgcn_sched_group_barrier(mask, n, 0)
__global__ __launch_bounds__(256) void k2_gemm1(const u16* __restrict__ hln_f, const u16* __restrict__ Wgu_f,
                                                u16* __restrict__ hh_f) {
  extern __shared__ __attribute__((aligned(16))) char smem[];
  const int tid = threadIdx.x;
  const int wave = tid >> 6, lane = tid & 63;
  const int bid = blockIdx.x;
  const int xcd = bid & 7, idx = bid >> 3;
  const int ft = (xcd & 1) * 7 + (idx % 7);
  const int rb = (xcd >> 1) * 4 + (idx / 7);

  STAMPW(3, 0);
  const char* asrc = (const char*)hln_f + (size_t)rb * 131072 + tid * 16;
  u32x4 ast[2][8];
#pragma unroll
  for (int i = 0; i < 8; ++i) ast[0][i] = *(const u32x4*)(asrc + i * 4096);
  const char* wbase = (const char*)Wgu_f + (size_t)((ft * 8 + wave * 2) * 2) * 16 * 1024 + lane * 16;
  constexpr int PF = 4;
  bf16x8 wf[PF + 1][4];
#pragma unroll
  for (int p = 0; p < PF; ++p)
#pragma unroll
    for (int j = 0; j < 4; ++j) wf[p][j] = *(const bf16x8*)(wbase + (j * 16 + p) * 1024);
#pragma unroll
  for (int i = 0; i < 8; ++i) ast[1][i] = *(const u32x4*)(asrc + 32768 + i * 4096);

  f32x4 acc[4][8];
#pragma unroll
  for (int j = 0; j < 4; ++j)
#pragma unroll
    for (int m = 0; m < 8; ++m) acc[j][m] = f32x4{0.f, 0.f, 0.f, 0.f};

  bf16x8 xf[2][8];
#pragma unroll
  for (int ch = 0; ch < 4; ++ch) {
    __builtin_amdgcn_sched_barrier(0);
#pragma unroll
    for (int i = 0; i < 8; ++i) *(u32x4*)(smem + ch * 32768 + i * 4096 + tid * 16) = ast[ch & 1][i];
    if (ch + 2 < 4) {
#pragma unroll
      for (int i = 0; i < 8; ++i) ast[ch & 1][i] = *(const u32x4*)(asrc + (ch + 2) * 32768 + i * 4096);
    }
    __syncthreads();
    if (ch == 0) STAMPW(3, 1);
#pragma unroll
    for (int m = 0; m < 8; ++m) xf[0][m] = *(const bf16x8*)(smem + (ch * 32 + m) * 1024 + lane * 16);
    __builtin_amdgcn_sched_barrier(0);
#pragma unroll
    for (int k4 = 0; k4 < 4; ++k4) {
      const int kb = ch * 4 + k4;
      if (kb + PF < 16) {
#pragma unroll
        for (int j = 0; j < 4; ++j)
          wf[(kb + PF) % (PF + 1)][j] = *(const bf16x8*)(wbase + (j * 16 + kb + PF) * 1024);
      }
      if (k4 + 1 < 4) {
#pragma unroll
        for (int m = 0; m < 8; ++m)
          xf[(k4 + 1) & 1][m] = *(const bf16x8*)(smem + ((kb + 1) * 8 + m) * 1024 + lane * 16);
      }
#pragma unroll
      for (int j = 0; j < 4; ++j)
#pragma unroll
        for (int m = 0; m < 8; ++m)
          acc[j][m] = __builtin_amdgcn_mfma_f32_16x16x32_bf16(wf[kb % (PF + 1)][j], xf[k4 & 1][m], acc[j][m], 0, 0, 0);
#pragma unroll
      for (int g = 0; g < 4; ++g) {
        SGB(0x8, 4); SGB(0x100, 1); SGB(0x8, 4); SGB(0x100, 1); SGB(0x20, 1);
      }
      __builtin_amdgcn_sched_barrier(0);
    }
  }
  STAMPW(3, 2);
  const int qd = lane >> 4, sl = lane & 15;
  char* obase = (char*)hh_f + (size_t)(ft * 4 + wave) * 1024 + ((qd >> 1) * 16 + sl) * 16 + (qd & 1) * 8;
#pragma unroll
  for (int fbl = 0; fbl < 2; ++fbl)
#pragma unroll
    for (int m = 0; m < 8; ++m) {
      float o[4];
#pragma unroll
      for (int r = 0; r < 4; ++r) {
        float gt = rbf(acc[fbl * 2][m][r]);
        float up = rbf(acc[fbl * 2 + 1][m][r]);
        gt = gt * (gt > 0.f ? 1.f : 0.f);
        o[r] = gt * up;
      }
      u32x2 pk = {pack2(o[0], o[1]), pack2(o[2], o[3])};
      *(u32x2*)(obase + (size_t)(rb * 8 + m) * 56 * 1024 + fbl * 512) = pk;
    }
  STAMPW(3, 3);
}

__global__ __launch_bounds__(256) void k3_gemm2(const u16* __restrict__ hh_f, const u16* __restrict__ Wd_f,
                                                const u16* __restrict__ hin, u16* __restrict__ out) {
  __shared__ __attribute__((aligned(16))) char red[4 * 16 * 1024];
  const int tid = threadIdx.x;
  const int wave = tid >> 6, lane = tid & 63;
  const int bid = blockIdx.x;
  const int xcd = bid & 7, idx = bid >> 3;
  const int dt = idx & 7, st = xcd * 4 + (idx >> 3);

  STAMPW(4, 0);
  const char* wb = (const char*)Wd_f + (size_t)(dt * 4 * 56 + wave * 14) * 1024 + lane * 16;
  const char* xb = (const char*)hh_f + (size_t)(st * 4 * 56 + wave * 14) * 1024 + lane * 16;
  constexpr int PF = 5;
  bf16x8 wf[PF + 1][4], xf[PF + 1][4];
#pragma unroll
  for (int p = 0; p < PF; ++p)
#pragma unroll
    for (int j = 0; j < 4; ++j) {
      wf[p][j] = *(const bf16x8*)(wb + (j * 56 + p) * 1024);
      xf[p][j] = *(const bf16x8*)(xb + (j * 56 + p) * 1024);
    }
  const int qd = lane >> 4, sl = lane & 15;
  u32x2 hv[4];
#pragma unroll
  for (int j = 0; j < 4; ++j)
    hv[j] = *(const u32x2*)(hin + (size_t)(st * 64 + j * 16 + sl) * D_ + dt * 64 + wave * 16 + qd * 4);
  f32x4 acc[4][4];
#pragma unroll
  for (int i = 0; i < 4; ++i)
#pragma unroll
    for (int j = 0; j < 4; ++j) acc[i][j] = f32x4{0.f, 0.f, 0.f, 0.f};
  __builtin_amdgcn_sched_barrier(0);
#pragma unroll
  for (int kb = 0; kb < 14; ++kb) {
    if (kb + PF < 14) {
#pragma unroll
      for (int j = 0; j < 4; ++j) {
        wf[(kb + PF) % (PF + 1)][j] = *(const bf16x8*)(wb + (j * 56 + kb + PF) * 1024);
        xf[(kb + PF) % (PF + 1)][j] = *(const bf16x8*)(xb + (j * 56 + kb + PF) * 1024);
      }
    }
#pragma unroll
    for (int i = 0; i < 4; ++i)
#pragma unroll
      for (int j = 0; j < 4; ++j)
        acc[i][j] = __builtin_amdgcn_mfma_f32_16x16x32_bf16(wf[kb % (PF + 1)][i], xf[kb % (PF + 1)][j], acc[i][j], 0, 0, 0);
#pragma unroll
    for (int g = 0; g < 8; ++g) { SGB(0x8, 2); SGB(0x20, 1); }
    __builtin_amdgcn_sched_barrier(0);
  }
  STAMPW(4, 1);
#pragma unroll
  for (int i = 0; i < 4; ++i)
#pragma unroll
    for (int j = 0; j < 4; ++j)
      *(f32x4*)(red + ((wave * 4 + i) * 4 + j) * 1024 + lane * 16) = acc[i][j];
  __syncthreads();
#pragma unroll
  for (int j = 0; j < 4; ++j) {
    f32x4 sum = f32x4{0.f, 0.f, 0.f, 0.f};
#pragma unroll
    for (int w = 0; w < 4; ++w) sum += *(const f32x4*)(red + ((w * 4 + wave) * 4 + j) * 1024 + lane * 16);
    float hx[4] = {bflo(hv[j][0]), bfhi(hv[j][0]), bflo(hv[j][1]), bfhi(hv[j][1])};
    float o[4];
#pragma unroll
    for (int r = 0; r < 4; ++r) o[r] = hx[r] + rbf(sum[r]);
    u32x2 pk = {pack2(o[0], o[1]), pack2(o[2], o[3])};
    *(u32x2*)(out + (size_t)(st * 64 + j * 16 + sl) * D_ + dt * 64 + wave * 16 + qd * 4) = pk;
  }
  STAMPW(4, 2);
}

extern "C" void kernel_launch(void* const* d_in, const int* in_sizes, int n_in, void* d_out, int out_size,
                              void* d_ws, size_t ws_size, hipStream_t stream) {
  const u16* q = (const u16*)d_in[0];
  const u16* k = (const u16*)d_in[1];
  const u16* v = (const u16*)d_in[2];
  const u16* shiftk = (const u16*)d_in[3];
  const u16* ssmk = (const u16*)d_in[4];
  const u16* ssmD = (const u16*)d_in[5];
  const u16* Wg = (const u16*)d_in[7];
  const u16* Wu = (const u16*)d_in[8];
  const u16* Wd = (const u16*)d_in[9];
  char* ws = (char*)d_ws;
  u16* y2T = (u16*)(ws);
  u16* hbuf = (u16*)(ws + (2u << 20));
  u16* hln_f = (u16*)(ws + (4u << 20));
  u16* Wgu_f = (u16*)(ws + (6u << 20));
  u16* Wd_f = (u16*)(ws + (10u << 20));
  u16* hh_f = (u16*)(ws + (12u << 20));
  u16* kvT = (u16*)(ws + (20u << 20));

  (void)hipFuncSetAttribute((const void*)k1_conv, hipFuncAttributeMaxDynamicSharedMemorySize, K1_LDS);
  (void)hipFuncSetAttribute((const void*)k2_gemm1, hipFuncAttributeMaxDynamicSharedMemorySize, 131072);

  k0_pre<<<256, 256, 0, stream>>>(k, v, shiftk, kvT);
  k1_conv<<<256, 256, K1_LDS, stream>>>(kvT, ssmk, ssmD, y2T, Wg, Wu, Wd, Wgu_f, Wd_f);
  k1b_rows<<<256, 256, 0, stream>>>(q, y2T, hbuf, hln_f);
  k2_gemm1<<<224, 256, 131072, stream>>>(hln_f, Wgu_f, hh_f);
  k3_gemm2<<<256, 256, 0, stream>>>(hh_f, Wd_f, hbuf, (u16*)d_out);
}
```

```cpp
#include <hip/hip_runtime.h>
#include <hip/hip_bf16.h>

typedef unsigned short u16;
typedef unsigned int u32;
using bf16x8 = __attribute__((ext_vector_type(8))) __bf16;
using f32x4 = __attribute__((ext_vector_type(4))) float;
using f32x16 = __attribute__((ext_vector_type(16))) float;
using u32x4 = __attribute__((ext_vector_type(4))) u32;
using u32x2 = __attribute__((ext_vector_type(2))) u32;
using s16x4 = __attribute__((ext_vector_type(4))) short;
#define LDS_PTR __attribute__((address_space(3)))

constexpr int S_ = 2048, D_ = 512, F_ = 1792;

#define STAMPW(kid, idx)

__device__ __forceinline__ float bf2f(u32 u) { return __uint_as_float(u << 16); }
__device__ __forceinline__ float bflo(u32 u) { return __uint_as_float(u << 16); }
__device__ __forceinline__ float bfhi(u32 u) { return __uint_as_float(u & 0xffff0000u); }
__device__ __forceinline__ u32 f2bf(float f) {
  __bf16 b = (__bf16)f;
  return (u32)__builtin_bit_cast(u16, b);
}
__device__ __forceinline__ float rbf(float f) { return bf2f(f2bf(f)); }
__device__ __forceinline__ u32 pack2(float lo, float hi) { return f2bf(lo) | (f2bf(hi) << 16); }

__global__ __launch_bounds__(256) void k0_pre(const u16* __restrict__ kin, const u16* __restrict__ vin,
                                              const u16* __restrict__ shiftk, u16* __restrict__ kvT) {
  const int tid = threadIdx.x;
  const int bid = blockIdx.x;
  STAMPW(0, 0);
  const int ct = bid & 7, rt = bid >> 3;
  const int d0 = ct * 64 + (tid & 31) * 2;
  const int r0 = rt * 64 + (tid >> 5) * 8;
  u32 kw[11], vw[8];
  {
    const u32* kp = (const u32*)(kin + d0);
    const u32* vp = (const u32*)(vin + d0);
#pragma unroll
    for (int i = 0; i < 11; ++i) {
      int r = r0 - 3 + i;
      kw[i] = (r >= 0) ? kp[(size_t)r * (D_ / 2)] : 0u;
    }
#pragma unroll
    for (int i = 0; i < 8; ++i) vw[i] = vp[(size_t)(r0 + i) * (D_ / 2)];
  }
  const u32x2 sw0 = *(const u32x2*)(shiftk + (size_t)d0 * 4);
  const u32x2 sw1 = *(const u32x2*)(shiftk + (size_t)(d0 + 1) * 4);
  {
    float w[2][4] = {{bflo(sw0[0]), bfhi(sw0[0]), bflo(sw0[1]), bfhi(sw0[1])},
                     {bflo(sw1[0]), bfhi(sw1[0]), bflo(sw1[1]), bfhi(sw1[1])}};
#pragma unroll
    for (int c = 0; c < 2; ++c) {
      float kf[11];
#pragma unroll
      for (int i = 0; i < 11; ++i) kf[i] = c ? bfhi(kw[i]) : bflo(kw[i]);
      float u[8];
#pragma unroll
      for (int i = 0; i < 8; ++i) {
        float acc = rbf(kf[i] * w[c][3]);
        acc = rbf(acc + rbf(kf[i + 1] * w[c][2]));
        acc = rbf(acc + rbf(kf[i + 2] * w[c][1]));
        acc = rbf(acc + rbf(kf[i + 3] * w[c][0]));
        float vf = c ? bfhi(vw[i]) : bflo(vw[i]);
        u[i] = acc * vf;
      }
      u32x4 pk = {pack2(u[0], u[1]), pack2(u[2], u[3]), pack2(u[4], u[5]), pack2(u[6], u[7])};
      *(u32x4*)(kvT + (size_t)(d0 + c) * S_ + r0) = pk;
    }
  }
  STAMPW(0, 1);
}

constexpr int KCS = 4160;
constexpr int KCH = 8 * KCS;
constexpr int UCH = 64 * 144;
constexpr int K1_KOFF = 0;
constexpr int K1_UOFF = 2 * KCH;
constexpr int K1_ROFF = K1_UOFF + 2 * UCH;
constexpr int K1_WOFF = K1_ROFF + 4 * 4096;
constexpr int K1_LDS = K1_WOFF + 4 * 32 * 272;

__device__ __forceinline__ u32 rot16(u32 x) { return (x >> 16) | (x << 16); }

__global__ __launch_bounds__(256) void k1_conv(const u16* __restrict__ kvT, const u16* __restrict__ ssmk,
                                               const u16* __restrict__ ssmD, u16* __restrict__ y2T,
                                               const u16* __restrict__ Wg, const u16* __restrict__ Wu,
                                               const u16* __restrict__ Wd, u16* __restrict__ Wgu_f,
                                               u16* __restrict__ Wd_f) {
  extern __shared__ __attribute__((aligned(16))) char smem[];
  const int tid = threadIdx.x;
  const int d0 = 2 * blockIdx.x;
  const int bid = blockIdx.x;
  STAMPW(1, 0);
  u32x4 g1[2], g0[2], uv[2];
#pragma unroll
  for (int c = 0; c < 2; ++c) {
    const u16* kr = ssmk + (size_t)(d0 + c) * S_;
    int a = 2040 - 8 * tid;
    g1[c] = *(const u32x4*)(kr + a);
    if (a >= 8) g0[c] = *(const u32x4*)(kr + a - 8);
    else g0[c] = u32x4{0, 0, 0, 0};
    uv[c] = *(const u32x4*)(kvT + (size_t)(d0 + c) * S_ + tid * 8);
  }
  const float dval0 = bf2f(ssmD[d0]), dval1 = bf2f(ssmD[d0 + 1]);
  const int wtask = bid * 4 + (tid >> 6);
  u32x4 rws[8];
  char* wdst = nullptr;
  int wstride = 0;
  if (wtask < 672) {
    const int which = wtask / 224, rem = wtask - which * 224;
    const u16* src;
    int ld, kb, nt;
    if (which < 2) {
      kb = rem / 14; nt = rem - kb * 14; ld = F_;
      src = (which == 0 ? Wg : Wu);
      wdst = (char*)Wgu_f + (size_t)((nt * 16 + which) * 16 + kb) * 1024;
      wstride = 32 * 1024;
    } else {
      kb = rem >> 2; nt = rem & 3; ld = D_;
      src = Wd;
      wdst = (char*)Wd_f + (size_t)(nt * 8 * 56 + kb) * 1024;
      wstride = 56 * 1024;
    }
    const int l = tid & 63;
    const u16* sp = src + (size_t)(kb * 32 + (l >> 4)) * ld + nt * 128 + (l & 15) * 8;
#pragma unroll
    for (int i = 0; i < 8; ++i) rws[i] = *(const u32x4*)(sp + (size_t)(4 * i) * ld);
  }
  {
    u32x4 z = {0, 0, 0, 0};
    for (int i = tid; i < 2 * 288; i += 256) {
      int c = i / 288, j = i - c * 288;
      *(u32x4*)(smem + K1_UOFF + c * UCH + j * 16) = z;
    }
    if (tid < 64) {
      int c = tid >> 5, r = (tid >> 2) & 7, q = 256 + (tid & 3);
      *(u32x4*)(smem + K1_KOFF + c * KCH + r * KCS + q * 16) = z;
    }
  }
  STAMPW(1, 1);
#pragma unroll
  for (int c = 0; c < 2; ++c) {
    u32 W[8], O[7];
#pragma unroll
    for (int i = 0; i < 4; ++i) {
      W[i] = rot16(g1[c][3 - i]);
      W[4 + i] = rot16(g0[c][3 - i]);
    }
#pragma unroll
    for (int i = 0; i < 7; ++i) O[i] = (W[i] >> 16) | (W[i + 1] << 16);
    char* base = smem + K1_KOFF + c * KCH + tid * 16;
#pragma unroll
    for (int r = 0; r < 8; ++r) {
      u32x4 val;
      if ((r & 1) == 0) val = u32x4{W[r / 2], W[r / 2 + 1], W[r / 2 + 2], W[r / 2 + 3]};
      else val = u32x4{O[r / 2], O[r / 2 + 1], O[r / 2 + 2], O[r / 2 + 3]};
      *(u32x4*)(base + r * KCS) = val;
    }
    *(u32x4*)(smem + K1_UOFF + c * UCH + (32 + (tid >> 3)) * 144 + (tid & 7) * 16) = uv[c];
  }
  __syncthreads();
  if (wtask < 672) {
    const int l = tid & 63;
    char* wl = smem + K1_WOFF + (tid >> 6) * (32 * 272);
#pragma unroll
    for (int i = 0; i < 8; ++i) *(u32x4*)(wl + (4 * i + (l >> 4)) * 272 + (l & 15) * 16) = rws[i];
    const char* rp = wl + (8 * (l >> 4) + ((l & 15) >> 2)) * 272 + (l & 3) * 8;
#pragma unroll
    for (int j = 0; j < 8; ++j) {
      s16x4 lo = __builtin_amdgcn_ds_read_tr16_b64_v4i16((LDS_PTR s16x4*)(rp + j * 32));
      s16x4 hi = __builtin_amdgcn_ds_read_tr16_b64_v4i16((LDS_PTR s16x4*)(rp + j * 32 + 4 * 272));
      u32x2 l2 = __builtin_bit_cast(u32x2, lo), h2 = __builtin_bit_cast(u32x2, hi);
      *(u32x4*)(wdst + (size_t)j * wstride + l * 16) = u32x4{l2[0], l2[1], h2[0], h2[1]};
    }
  }
  STAMPW(1, 2);

  const int wave = tid >> 6, lane = tid & 63;
  const int c = wave >> 1, hf = wave & 1;
  const int b = lane & 31, h = lane >> 5;
  f32x16 acc0, acc1;
#pragma unroll
  for (int i = 0; i < 16; ++i) { acc0[i] = 0.f; acc1[i] = 0.f; }
  {
    const int i0 = 2047 - (-16 + 1024 * hf) - b + 8 * h;
    const int r = i0 & 7;
    const char* ap = smem + K1_KOFF + c * KCH + r * KCS + (i0 - r) * 2 - 32 * 63;
    const char* ub = smem + K1_UOFF + c * UCH + (32 + b - 16 * hf - 16) * 144 + 16 * h;
    auto boff = [](int j) -> int {
      int base = 16 - 16 * j;
      int blkoff = (base >= 0) ? (base >> 6) : -((-base + 63) >> 6);
      int e0 = base - blkoff * 64;
      return (blkoff + 16) * 144 + e0 * 2;
    };
    constexpr int PD = 3;
    bf16x8 af[PD + 1], bfr[PD + 3];
#pragma unroll
    for (int j = -2; j < PD; ++j) bfr[(j + 2) % (PD + 3)] = *(const bf16x8*)(ub + boff(j));
#pragma unroll
    for (int i = 0; i < PD; ++i) af[i % (PD + 1)] = *(const bf16x8*)(ap + 32 * (63 - i));
#pragma unroll
    for (int i = 0; i < 64; ++i) {
      if (i + PD < 64) {
        af[(i + PD) % (PD + 1)] = *(const bf16x8*)(ap + 32 * (63 - i - PD));
        bfr[(i + PD + 2) % (PD + 3)] = *(const bf16x8*)(ub + boff(i + PD));
      }
      acc0 = __builtin_amdgcn_mfma_f32_32x32x16_bf16(af[i % (PD + 1)], bfr[(i + 2) % (PD + 3)], acc0, 0, 0, 0);
      acc1 = __builtin_amdgcn_mfma_f32_32x32x16_bf16(af[i % (PD + 1)], bfr[i % (PD + 3)], acc1, 0, 0, 0);
      __builtin_amdgcn_sched_group_barrier(0x8, 1, 0);
      __builtin_amdgcn_sched_group_barrier(0x100, 1, 0);
      __builtin_amdgcn_sched_group_barrier(0x8, 1, 0);
      __builtin_amdgcn_sched_group_barrier(0x100, 1, 0);
      __builtin_amdgcn_sched_barrier(0);
    }
  }
  STAMPW(1, 3);
  {
    float* mine = (float*)(smem + K1_ROFF + wave * 4096);
    const float* other = (const float*)(smem + K1_ROFF + (wave ^ 1) * 4096);
#pragma unroll
    for (int i = 0; i < 4; ++i) {
      f32x4 x;
#pragma unroll
      for (int j = 0; j < 4; ++j) x[j] = hf ? acc0[4 * i + j] : acc1[4 * i + j];
      *(f32x4*)(mine + (i * 64 + lane) * 4) = x;
    }
    __syncthreads();
    f32x16 acc = hf ? acc1 : acc0;
#pragma unroll
    for (int i = 0; i < 4; ++i) {
      f32x4 x = *(const f32x4*)(other + (i * 64 + lane) * 4);
#pragma unroll
      for (int j = 0; j < 4; ++j) acc[4 * i + j] += x[j];
    }
    const float dval = c ? dval1 : dval0;
    u16* orow = y2T + (size_t)(d0 + c) * 8 + 4 * h;
#pragma unroll
    for (int i = 0; i < 4; ++i) {
      int e = 32 * hf + 8 * i + 4 * h;
      u32x2 kvp = *(const u32x2*)(smem + K1_UOFF + c * UCH + (32 + b) * 144 + e * 2);
      float kv[4] = {bflo(kvp[0]), bfhi(kvp[0]), bflo(kvp[1]), bfhi(kvp[1])};
      float o[4];
#pragma unroll
      for (int j = 0; j < 4; ++j) o[j] = rbf(acc[4 * i + j]) + rbf(kv[j] * dval);
      u32x2 pk = {pack2(o[0], o[1]), pack2(o[2], o[3])};
      *(u32x2*)(orow + (size_t)(8 * b + 4 * hf + i) * (D_ * 8)) = pk;
    }
  }
  STAMPW(1, 4);
}

template <int CTRL>
__device__ __forceinline__ float dpp_add(float x) {
  int y = __builtin_amdgcn_update_dpp(0, __float_as_int(x), CTRL, 0xf, 0xf, true);
  return x + __int_as_float(y);
}
__device__ __forceinline__ float wave_sum(float x) {
  x = dpp_add<0xB1>(x);
  x = dpp_add<0x4E>(x);
  x = dpp_add<0x141>(x);
  x = dpp_add<0x140>(x);
  int xi = __float_as_int(x);
  float a = __int_as_float(__builtin_amdgcn_readlane(xi, 0));
  float b = __int_as_float(__builtin_amdgcn_readlane(xi, 16));
  float c = __int_as_float(__builtin_amdgcn_readlane(xi, 32));
  float d = __int_as_float(__builtin_amdgcn_readlane(xi, 48));
  return (a + b) + (c + d);
}

__device__ __forceinline__ void ln_bf16x2(const float (&x)[2][8], float (&o)[2][8]) {
  float s[2], ss[2], mean[2];
#pragma unroll
  for (int r = 0; r < 2; ++r) {
    s[r] = ((x[r][0] + x[r][1]) + (x[r][2] + x[r][3])) + ((x[r][4] + x[r][5]) + (x[r][6] + x[r][7]));
  }
#pragma unroll
  for (int r = 0; r < 2; ++r) mean[r] = wave_sum(s[r]) * (1.f / 512.f);
#pragma unroll
  for (int r = 0; r < 2; ++r) {
    ss[r] = 0.f;
#pragma unroll
    for (int i = 0; i < 8; ++i) { float dlt = x[r][i] - mean[r]; ss[r] += dlt * dlt; }
  }
#pragma unroll
  for (int r = 0; r < 2; ++r) {
    const float var = wave_sum(ss[r]) * (1.f / 512.f);
    const float mb = rbf(mean[r]);
    const float vb = rbf(var);
    const float sd = rbf(sqrtf(rbf(vb + 1e-5f)));
    const float rc = 1.0f / sd;
#pragma unroll
    for (int i = 0; i < 8; ++i) {
      float a = rbf(x[r][i] - mb);
      float qt = a * rc;
      float rem = __builtin_fmaf(-qt, sd, a);
      qt = __builtin_fmaf(rem, rc, qt);
      o[r][i] = rbf(qt);
    }
  }
}

__global__ __launch_bounds__(256) void k1b_rows(const u16* __restrict__ q, const u16* __restrict__ y2T,
                                                u16* __restrict__ hout, u16* __restrict__ hln_f) {
  __shared__ __attribute__((aligned(16))) u32 tile[8][264];
  const int tid = threadIdx.x;
  const int s0 = blockIdx.x * 8;
  const int wave = tid >> 6, lane = tid & 63;
  STAMPW(2, 0);
  u32x4 ya = *(const u32x4*)(y2T + (size_t)blockIdx.x * (D_ * 8) + (2 * tid) * 8);
  u32x4 yb = *(const u32x4*)(y2T + (size_t)blockIdx.x * (D_ * 8) + (2 * tid + 1) * 8);
  u32x4 qv[2];
#pragma unroll
  for (int r = 0; r < 2; ++r) qv[r] = *(const u32x4*)(q + (size_t)(s0 + 2 * wave + r) * D_ + lane * 8);
  STAMPW(2, 1);
#pragma unroll
  for (int i = 0; i < 4; ++i) {
    tile[2 * i][tid] = (ya[i] & 0xffffu) | (yb[i] << 16);
    tile[2 * i + 1][tid] = (ya[i] >> 16) | (yb[i] & 0xffff0000u);
  }
  float x[2][8], ql[2][8];
#pragma unroll
  for (int r = 0; r < 2; ++r)
#pragma unroll
    for (int i = 0; i < 4; ++i) {
      x[r][2 * i] = bflo(qv[r][i]);
      x[r][2 * i + 1] = bfhi(qv[r][i]);
    }
  ln_bf16x2(x, ql);
  __syncthreads();
  float hh[2][8], hl[2][8];
#pragma unroll
  for (int r = 0; r < 2; ++r) {
    u32x4 yv = *(const u32x4*)(&tile[2 * wave + r][lane * 4]);
#pragma unroll
    for (int i = 0; i < 4; ++i) {
      hh[r][2 * i] = rbf(x[r][2 * i] + rbf(ql[r][2 * i] * bflo(yv[i])));
      hh[r][2 * i + 1] = rbf(x[r][2 * i + 1] + rbf(ql[r][2 * i + 1] * bfhi(yv[i])));
    }
  }
  ln_bf16x2(hh, hl);
#pragma unroll
  for (int r = 0; r < 2; ++r) {
    const int s = s0 + 2 * wave + r;
    u32x4 hp = {pack2(hh[r][0], hh[r][1]), pack2(hh[r][2], hh[r][3]), pack2(hh[r][4], hh[r][5]),
                pack2(hh[r][6], hh[r][7])};
    u32x4 lp = {pack2(hl[r][0], hl[r][1]), pack2(hl[r][2], hl[r][3]), pack2(hl[r][4], hl[r][5]),
                pack2(hl[r][6], hl[r][7])};
    *(u32x4*)(hout + (size_t)s * D_ + lane * 8) = hp;
    char* dst = (char*)hln_f + (size_t)(((s >> 7) * 16 + (lane >> 2)) * 8 + ((s >> 4) & 7)) * 1024 +
                ((lane & 3) * 16 + (s & 15)) * 16;
    *(u32x4*)dst = lp;
  }
  STAMPW(2, 2);
}

#define SGB(mask, n) __builtin_amdgcn_sched_group_barrier(mask, n, 0)
__global__ __launch_bounds__(256) void k2_gemm1(const u16* __restrict__ hln_f, const u16* __restrict__ Wgu_f,
                                                u16* __restrict__ hh_f) {
  extern __shared__ __attribute__((aligned(16))) char smem[];
  const int tid = threadIdx.x;
  const int wave = tid >> 6, lane = tid & 63;
  const int bid = blockIdx.x;
  const int xcd = bid & 7, idx = bid >> 3;
  const int ft = (xcd & 1) * 7 + (idx % 7);
  const int rb = (xcd >> 1) * 4 + (idx / 7);

  STAMPW(3, 0);
  const char* asrc = (const char*)hln_f + (size_t)rb * 131072 + tid * 16;
  u32x4 ast[2][8];
#pragma unroll
  for (int i = 0; i < 8; ++i) ast[0][i] = *(const u32x4*)(asrc + i * 4096);
  const char* wbase = (const char*)Wgu_f + (size_t)((ft * 8 + wave * 2) * 2) * 16 * 1024 + lane * 16;
  constexpr int PF = 4;
  bf16x8 wf[PF + 1][4];
#pragma unroll
  for (int p = 0; p < PF; ++p)
#pragma unroll
    for (int j = 0; j < 4; ++j) wf[p][j] = *(const bf16x8*)(wbase + (j * 16 + p) * 1024);
#pragma unroll
  for (int i = 0; i < 8; ++i) ast[1][i] = *(const u32x4*)(asrc + 32768 + i * 4096);

  f32x4 acc[4][8];
#pragma unroll
  for (int j = 0; j < 4; ++j)
#pragma unroll
    for (int m = 0; m < 8; ++m) acc[j][m] = f32x4{0.f, 0.f, 0.f, 0.f};

  bf16x8 xf[2][8];
#pragma unroll
  for (int ch = 0; ch < 4; ++ch) {
    __builtin_amdgcn_sched_barrier(0);
#pragma unroll
    for (int i = 0; i < 8; ++i) *(u32x4*)(smem + ch * 32768 + i * 4096 + tid * 16) = ast[ch & 1][i];
    if (ch + 2 < 4) {
#pragma unroll
      for (int i = 0; i < 8; ++i) ast[ch & 1][i] = *(const u32x4*)(asrc + (ch + 2) * 32768 + i * 4096);
    }
    __syncthreads();
    if (ch == 0) STAMPW(3, 1);
#pragma unroll
    for (int m = 0; m < 8; ++m) xf[0][m] = *(const bf16x8*)(smem + (ch * 32 + m) * 1024 + lane * 16);
    __builtin_amdgcn_sched_barrier(0);
#pragma unroll
    for (int k4 = 0; k4 < 4; ++k4) {
      const int kb = ch * 4 + k4;
      if (kb + PF < 16) {
#pragma unroll
        for (int j = 0; j < 4; ++j)
          wf[(kb + PF) % (PF + 1)][j] = *(const bf16x8*)(wbase + (j * 16 + kb + PF) * 1024);
      }
      if (k4 + 1 < 4) {
#pragma unroll
        for (int m = 0; m < 8; ++m)
          xf[(k4 + 1) & 1][m] = *(const bf16x8*)(smem + ((kb + 1) * 8 + m) * 1024 + lane * 16);
      }
#pragma unroll
      for (int j = 0; j < 4; ++j)
#pragma unroll
        for (int m = 0; m < 8; ++m)
          acc[j][m] = __builtin_amdgcn_mfma_f32_16x16x32_bf16(wf[kb % (PF + 1)][j], xf[k4 & 1][m], acc[j][m], 0, 0, 0);
#pragma unroll
      for (int g = 0; g < 4; ++g) {
        SGB(0x8, 4); SGB(0x100, 1); SGB(0x8, 4); SGB(0x100, 1); SGB(0x20, 1);
      }
      __builtin_amdgcn_sched_barrier(0);
    }
  }
  STAMPW(3, 2);
  const int qd = lane >> 4, sl = lane & 15;
  char* obase = (char*)hh_f + (size_t)(ft * 4 + wave) * 1024 + ((qd >> 1) * 16 + sl) * 16 + (qd & 1) * 8;
#pragma unroll
  for (int fbl = 0; fbl < 2; ++fbl)
#pragma unroll
    for (int m = 0; m < 8; ++m) {
      float o[4];
#pragma unroll
      for (int r = 0; r < 4; ++r) {
        float gt = rbf(acc[fbl * 2][m][r]);
        float up = rbf(acc[fbl * 2 + 1][m][r]);
        gt = gt * (gt > 0.f ? 1.f : 0.f);
        o[r] = gt * up;
      }
      u32x2 pk = {pack2(o[0], o[1]), pack2(o[2], o[3])};
      *(u32x2*)(obase + (size_t)(rb * 8 + m) * 56 * 1024 + fbl * 512) = pk;
    }
  STAMPW(3, 3);
}

__global__ __launch_bounds__(256) void k3_gemm2(const u16* __restrict__ hh_f, const u16* __restrict__ Wd_f,
                                                const u16* __restrict__ hin, u16* __restrict__ out) {
  __shared__ __attribute__((aligned(16))) char red[4 * 16 * 1024];
  const int tid = threadIdx.x;
  const int wave = tid >> 6, lane = tid & 63;
  const int bid = blockIdx.x;
  const int xcd = bid & 7, idx = bid >> 3;
  const int dt = idx & 7, st = xcd * 4 + (idx >> 3);

  STAMPW(4, 0);
  const char* wb = (const char*)Wd_f + (size_t)(dt * 4 * 56 + wave * 14) * 1024 + lane * 16;
  const char* xb = (const char*)hh_f + (size_t)(st * 4 * 56 + wave * 14) * 1024 + lane * 16;
  constexpr int PF = 5;
  bf16x8 wf[PF + 1][4], xf[PF + 1][4];
#pragma unroll
  for (int p = 0; p < PF; ++p)
#pragma unroll
    for (int j = 0; j < 4; ++j) {
      wf[p][j] = *(const bf16x8*)(wb + (j * 56 + p) * 1024);
      xf[p][j] = *(const bf16x8*)(xb + (j * 56 + p) * 1024);
    }
  const int qd = lane >> 4, sl = lane & 15;
  u32x2 hv[4];
#pragma unroll
  for (int j = 0; j < 4; ++j)
    hv[j] = *(const u32x2*)(hin + (size_t)(st * 64 + j * 16 + sl) * D_ + dt * 64 + wave * 16 + qd * 4);
  f32x4 acc[4][4];
#pragma unroll
  for (int i = 0; i < 4; ++i)
#pragma unroll
    for (int j = 0; j < 4; ++j) acc[i][j] = f32x4{0.f, 0.f, 0.f, 0.f};
  __builtin_amdgcn_sched_barrier(0);
#pragma unroll
  for (int kb = 0; kb < 14; ++kb) {
    if (kb + PF < 14) {
#pragma unroll
      for (int j = 0; j < 4; ++j) {
        wf[(kb + PF) % (PF + 1)][j] = *(const bf16x8*)(wb + (j * 56 + kb + PF) * 1024);
        xf[(kb + PF) % (PF + 1)][j] = *(const bf16x8*)(xb + (j * 56 + kb + PF) * 1024);
      }
    }
#pragma unroll
    for (int i = 0; i < 4; ++i)
#pragma unroll
      for (int j = 0; j < 4; ++j)
        acc[i][j] = __builtin_amdgcn_mfma_f32_16x16x32_bf16(wf[kb % (PF + 1)][i], xf[kb % (PF + 1)][j], acc[i][j], 0, 0, 0);
#pragma unroll
    for (int g = 0; g < 8; ++g) { SGB(0x8, 2); SGB(0x20, 1); }
    __builtin_amdgcn_sched_barrier(0);
  }
  STAMPW(4, 1);
#pragma unroll
  for (int i = 0; i < 4; ++i)
#pragma unroll
    for (int j = 0; j < 4; ++j)
      *(f32x4*)(red + ((wave * 4 + i) * 4 + j) * 1024 + lane * 16) = acc[i][j];
  __syncthreads();
#pragma unroll
  for (int j = 0; j < 4; ++j) {
    f32x4 sum = f32x4{0.f, 0.f, 0.f, 0.f};
#pragma unroll
    for (int w = 0; w < 4; ++w) sum += *(const f32x4*)(red + ((w * 4 + wave) * 4 + j) * 1024 + lane * 16);
    float hx[4] = {bflo(hv[j][0]), bfhi(hv[j][0]), bflo(hv[j][1]), bfhi(hv[j][1])};
    float o[4];
#pragma unroll
    for (int r = 0; r < 4; ++r) o[r] = hx[r] + rbf(sum[r]);
    u32x2 pk = {pack2(o[0], o[1]), pack2(o[2], o[3])};
    *(u32x2*)(out + (size_t)(st * 64 + j * 16 + sl) * D_ + dt * 64 + wave * 16 + qd * 4) = pk;
  }
  STAMPW(4, 2);
}

extern "C" void kernel_launch(void* const* d_in, const int* in_sizes, int n_in, void* d_out, int out_size,
                              void* d_ws, size_t ws_size, hipStream_t stream) {
  const u16* q = (const u16*)d_in[0];
  const u16* k = (const u16*)d_in[1];
  const u16* v = (const u16*)d_in[2];
  const u16* shiftk = (const u16*)d_in[3];
  const u16* ssmk = (const u16*)d_in[4];
  const u16* ssmD = (const u16*)d_in[5];
  const u16* Wg = (const u16*)d_in[7];
  const u16* Wu = (const u16*)d_in[8];
  const u16* Wd = (const u16*)d_in[9];
  char* ws = (char*)d_ws;
  u16* y2T = (u16*)(ws);
  u16* hbuf = (u16*)(ws + (2u << 20));
  u16* hln_f = (u16*)(ws + (4u << 20));
  u16* Wgu_f = (u16*)(ws + (6u << 20));
  u16* Wd_f = (u16*)(ws + (10u << 20));
  u16* hh_f = (u16*)(ws + (12u << 20));
  u16* kvT = (u16*)(ws + (20u << 20));

  (void)hipFuncSetAttribute((const void*)k1_conv, hipFuncAttributeMaxDynamicSharedMemorySize, K1_LDS);
  (void)hipFuncSetAttribute((const void*)k2_gemm1, hipFuncAttributeMaxDynamicSharedMemorySize, 131072);

  k0_pre<<<256, 256, 0, stream>>>(k, v, shiftk, kvT);
  k1_conv<<<256, 256, K1_LDS, stream>>>(kvT, ssmk, ssmD, y2T, Wg, Wu, Wd, Wgu_f, Wd_f);
  k1b_rows<<<256, 256, 0, stream>>>(q, y2T, hbuf, hln_f);
  k2_gemm1<<<224, 256, 131072, stream>>>(hln_f, Wgu_f, hh_f);
  k3_gemm2<<<256, 256, 0, stream>>>(hh_f, Wd_f, hbuf, (u16*)d_out);
}
```

```cpp
#include <hip/hip_runtime.h>
#include <hip/hip_bf16.h>

typedef unsigned short u16;
typedef unsigned int u32;
using bf16x8 = __attribute__((ext_vector_type(8))) __bf16;
using f32x4 = __attribute__((ext_vector_type(4))) float;
using f32x16 = __attribute__((ext_vector_type(16))) float;
using u32x4 = __attribute__((ext_vector_type(4))) u32;
using u32x2 = __attribute__((ext_vector_type(2))) u32;
using s16x4 = __attribute__((ext_vector_type(4))) short;
#define LDS_PTR __attribute__((address_space(3)))

constexpr int S_ = 2048, D_ = 512, F_ = 1792;

#define STAMPW(kid, idx)

__device__ __forceinline__ float bf2f(u32 u) { return __uint_as_float(u << 16); }
__device__ __forceinline__ float bflo(u32 u) { return __uint_as_float(u << 16); }
__device__ __forceinline__ float bfhi(u32 u) { return __uint_as_float(u & 0xffff0000u); }
__device__ __forceinline__ u32 f2bf(float f) {
  __bf16 b = (__bf16)f;
  return (u32)__builtin_bit_cast(u16, b);
}
__device__ __forceinline__ float rbf(float f) { return bf2f(f2bf(f)); }
__device__ __forceinline__ u32 pack2(float lo, float hi) { return f2bf(lo) | (f2bf(hi) << 16); }

__global__ __launch_bounds__(256) void k0_pre(const u16* __restrict__ kin, const u16* __restrict__ vin,
                                              const u16* __restrict__ shiftk, u16* __restrict__ kvT) {
  const int tid = threadIdx.x;
  const int bid = blockIdx.x;
  STAMPW(0, 0);
  const int ct = bid & 7, rt = bid >> 3;
  const int d0 = ct * 64 + (tid & 31) * 2;
  const int r0 = rt * 64 + (tid >> 5) * 8;
  u32 kw[11], vw[8];
  {
    const u32* kp = (const u32*)(kin + d0);
    const u32* vp = (const u32*)(vin + d0);
#pragma unroll
    for (int i = 0; i < 11; ++i) {
      int r = r0 - 3 + i;
      kw[i] = (r >= 0) ? kp[(size_t)r * (D_ / 2)] : 0u;
    }
#pragma unroll
    for (int i = 0; i < 8; ++i) vw[i] = vp[(size_t)(r0 + i) * (D_ / 2)];
  }
  const u32x2 sw0 = *(const u32x2*)(shiftk + (size_t)d0 * 4);
  const u32x2 sw1 = *(const u32x2*)(shiftk + (size_t)(d0 + 1) * 4);
  {
    float w[2][4] = {{bflo(sw0[0]), bfhi(sw0[0]), bflo(sw0[1]), bfhi(sw0[1])},
                     {bflo(sw1[0]), bfhi(sw1[0]), bflo(sw1[1]), bfhi(sw1[1])}};
#pragma unroll
    for (int c = 0; c < 2; ++c) {
      float kf[11];
#pragma unroll
      for (int i = 0; i < 11; ++i) kf[i] = c ? bfhi(kw[i]) : bflo(kw[i]);
      float u[8];
#pragma unroll
      for (int i = 0; i < 8; ++i) {
        float acc = rbf(kf[i] * w[c][3]);
        acc = rbf(acc + rbf(kf[i + 1] * w[c][2]));
        acc = rbf(acc + rbf(kf[i + 2] * w[c][1]));
        acc = rbf(acc + rbf(kf[i + 3] * w[c][0]));
        float vf = c ? bfhi(vw[i]) : bflo(vw[i]);
        u[i] = acc * vf;
      }
      u32x4 pk = {pack2(u[0], u[1]), pack2(u[2], u[3]), pack2(u[4], u[5]), pack2(u[6], u[7])};
      *(u32x4*)(kvT + (size_t)(d0 + c) * S_ + r0) = pk;
    }
  }
  STAMPW(0, 1);
}

constexpr int KCS = 4160;
constexpr int KCH = 8 * KCS;
constexpr int UBS = 80;
constexpr int UCH = 96 * UBS;
constexpr int K1_KOFF = 0;
constexpr int K1_UOFF = 2 * KCH;
constexpr int K1_ROFF = K1_UOFF + 2 * UCH;
constexpr int K1_WOFF = K1_ROFF + 2 * 4096;
constexpr int K1_LDS = K1_WOFF + 3 * 32 * 272;

__device__ __forceinline__ u32 rot16(u32 x) { return (x >> 16) | (x << 16); }

__device__ __forceinline__ constexpr int k1_boff(int j) {
  return ((((16 - 16 * j) + 2048) >> 5) - 64 + 32) * UBS + (((16 - 16 * j) + 2048) & 31) * 2;
}

__global__ __launch_bounds__(448) void k1_conv(const u16* __restrict__ kvT, const u16* __restrict__ ssmk,
                                               const u16* __restrict__ ssmD, u16* __restrict__ y2T,
                                               const u16* __restrict__ Wg, const u16* __restrict__ Wu,
                                               const u16* __restrict__ Wd, u16* __restrict__ Wgu_f,
                                               u16* __restrict__ Wd_f) {
  extern __shared__ __attribute__((aligned(16))) char smem[];
  const int tid = threadIdx.x;
  const int d0 = 2 * blockIdx.x;
  const int bid = blockIdx.x;
  const int wave = __builtin_amdgcn_readfirstlane(tid >> 6), lane = tid & 63;
  STAMPW(1, 0);
  if (wave >= 4) {
    const int wtask = bid * 3 + (wave - 4);
    u32x4 rws[8];
    char* wdst = nullptr;
    int wstride = 0;
    if (wtask < 672) {
      const int which = wtask / 224, rem = wtask - which * 224;
      const u16* src;
      int ld, kb, nt;
      if (which < 2) {
        kb = rem / 14; nt = rem - kb * 14; ld = F_;
        src = (which == 0 ? Wg : Wu);
        wdst = (char*)Wgu_f + (size_t)((nt * 16 + which) * 16 + kb) * 1024;
        wstride = 32 * 1024;
      } else {
        kb = rem >> 2; nt = rem & 3; ld = D_;
        src = Wd;
        wdst = (char*)Wd_f + (size_t)(nt * 8 * 56 + kb) * 1024;
        wstride = 56 * 1024;
      }
      const u16* sp = src + (size_t)(kb * 32 + (lane >> 4)) * ld + nt * 128 + (lane & 15) * 8;
#pragma unroll
      for (int i = 0; i < 8; ++i) rws[i] = *(const u32x4*)(sp + (size_t)(4 * i) * ld);
    }
    __builtin_amdgcn_s_barrier();
    if (wtask < 672) {
      char* wl = smem + K1_WOFF + (wave - 4) * (32 * 272);
#pragma unroll
      for (int i = 0; i < 8; ++i) *(u32x4*)(wl + (4 * i + (lane >> 4)) * 272 + (lane & 15) * 16) = rws[i];
      const char* rp = wl + (8 * (lane >> 4) + ((lane & 15) >> 2)) * 272 + (lane & 3) * 8;
#pragma unroll
      for (int j = 0; j < 8; ++j) {
        s16x4 lo = __builtin_amdgcn_ds_read_tr16_b64_v4i16((LDS_PTR s16x4*)(rp + j * 32));
        s16x4 hi = __builtin_amdgcn_ds_read_tr16_b64_v4i16((LDS_PTR s16x4*)(rp + j * 32 + 4 * 272));
        u32x2 l2 = __builtin_bit_cast(u32x2, lo), h2 = __builtin_bit_cast(u32x2, hi);
        *(u32x4*)(wdst + (size_t)j * wstride + lane * 16) = u32x4{l2[0], l2[1], h2[0], h2[1]};
      }
    }
    __builtin_amdgcn_s_barrier();
    return;
  }
  u32x4 g1[2], g0[2], uv[2];
#pragma unroll
  for (int c = 0; c < 2; ++c) {
    const u16* kr = ssmk + (size_t)(d0 + c) * S_;
    int a = 2040 - 8 * tid;
    g1[c] = *(const u32x4*)(kr + a);
    if (a >= 8) g0[c] = *(const u32x4*)(kr + a - 8);
    else g0[c] = u32x4{0, 0, 0, 0};
    uv[c] = *(const u32x4*)(kvT + (size_t)(d0 + c) * S_ + tid * 8);
  }
  const float dval0 = bf2f(ssmD[d0]), dval1 = bf2f(ssmD[d0 + 1]);
  {
    u32x4 z = {0, 0, 0, 0};
    for (int i = tid; i < 2 * 160; i += 256) {
      int c = i / 160, j = i - c * 160;
      *(u32x4*)(smem + K1_UOFF + c * UCH + j * 16) = z;
    }
    if (tid < 64) {
      int c = tid >> 5, r = (tid >> 2) & 7, q = 256 + (tid & 3);
      *(u32x4*)(smem + K1_KOFF + c * KCH + r * KCS + q * 16) = z;
    }
  }
  STAMPW(1, 1);
#pragma unroll
  for (int c = 0; c < 2; ++c) {
    u32 W[8], O[7];
#pragma unroll
    for (int i = 0; i < 4; ++i) {
      W[i] = rot16(g1[c][3 - i]);
      W[4 + i] = rot16(g0[c][3 - i]);
    }
#pragma unroll
    for (int i = 0; i < 7; ++i) O[i] = (W[i] >> 16) | (W[i + 1] << 16);
    char* base = smem + K1_KOFF + c * KCH + tid * 16;
#pragma unroll
    for (int r = 0; r < 8; ++r) {
      u32x4 val;
      if ((r & 1) == 0) val = u32x4{W[r / 2], W[r / 2 + 1], W[r / 2 + 2], W[r / 2 + 3]};
      else val = u32x4{O[r / 2], O[r / 2 + 1], O[r / 2 + 2], O[r / 2 + 3]};
      *(u32x4*)(base + r * KCS) = val;
    }
    *(u32x4*)(smem + K1_UOFF + c * UCH + (32 + (tid >> 2)) * UBS + (tid & 3) * 16) = uv[c];
  }
  __syncthreads();
  STAMPW(1, 2);

  const int c = wave >> 1, hf = wave & 1;
  const int b = lane & 31, h = lane >> 5;
  f32x16 acc0, acc1;
#pragma unroll
  for (int i = 0; i < 16; ++i) { acc0[i] = 0.f; acc1[i] = 0.f; }
  const int i0 = 2047 + 16 - b + 8 * h;
  const int r = i0 & 7;
  const char* abase = smem + K1_KOFF + c * KCH + r * KCS + (i0 - r) * 2;
  const char* ulo = smem + K1_UOFF + c * UCH + b * UBS + 16 * h;
  const char* uhi = ulo + 32 * UBS;
  constexpr int PD = 3;
  if (hf == 0) {
    const char* ap = abase - 32 * 63;
    bf16x8 af[PD + 1], bl[PD + 1], bh[PD + 1];
#pragma unroll
    for (int j = 0; j < PD; ++j) {
      af[j] = *(const bf16x8*)(ap + 32 * (63 - j));
      bl[j] = *(const bf16x8*)(ulo + k1_boff(j));
      bh[j] = *(const bf16x8*)(uhi + k1_boff(j));
    }
#pragma unroll
    for (int j = 0; j < 64; ++j) {
      if (j + PD < 64) {
        af[(j + PD) % (PD + 1)] = *(const bf16x8*)(ap + 32 * (63 - j - PD));
        bl[(j + PD) % (PD + 1)] = *(const bf16x8*)(ulo + k1_boff(j + PD));
        if (j + PD < 32) bh[(j + PD) % (PD + 1)] = *(const bf16x8*)(uhi + k1_boff(j + PD));
      }
      acc0 = __builtin_amdgcn_mfma_f32_32x32x16_bf16(af[j % (PD + 1)], bl[j % (PD + 1)], acc0, 0, 0, 0);
      if (j < 32) acc1 = __builtin_amdgcn_mfma_f32_32x32x16_bf16(af[j % (PD + 1)], bh[j % (PD + 1)], acc1, 0, 0, 0);
      __builtin_amdgcn_sched_group_barrier(0x8, 1, 0);
      __builtin_amdgcn_sched_group_barrier(0x100, 2, 0);
      __builtin_amdgcn_sched_group_barrier(0x8, 1, 0);
      __builtin_amdgcn_sched_group_barrier(0x100, 1, 0);
      __builtin_amdgcn_sched_barrier(0);
    }
  } else {
    const char* ap = abase - 32 * 127;
    bf16x8 af[PD + 1], bh[PD + 1];
#pragma unroll
    for (int j = 0; j < PD; ++j) {
      af[j] = *(const bf16x8*)(ap + 32 * (127 - 32 - j));
      bh[j] = *(const bf16x8*)(uhi + k1_boff(32 + j));
    }
#pragma unroll
    for (int j = 0; j < 96; ++j) {
      if (j + PD < 96) {
        af[(j + PD) % (PD + 1)] = *(const bf16x8*)(ap + 32 * (127 - 32 - j - PD));
        bh[(j + PD) % (PD + 1)] = *(const bf16x8*)(uhi + k1_boff(32 + j + PD));
      }
      acc0 = __builtin_amdgcn_mfma_f32_32x32x16_bf16(af[j % (PD + 1)], bh[j % (PD + 1)], acc0, 0, 0, 0);
      __builtin_amdgcn_sched_group_barrier(0x8, 1, 0);
      __builtin_amdgcn_sched_group_barrier(0x100, 2, 0);
      __builtin_amdgcn_sched_barrier(0);
    }
  }
  STAMPW(1, 3);
  {
    float* xch = (float*)(smem + K1_ROFF + c * 4096);
    if (hf == 0) {
#pragma unroll
      for (int i = 0; i < 4; ++i) {
        f32x4 x;
#pragma unroll
        for (int j = 0; j < 4; ++j) x[j] = acc1[4 * i + j];
        *(f32x4*)(xch + (i * 64 + lane) * 4) = x;
      }
    }
    __syncthreads();
    if (hf == 1) {
#pragma unroll
      for (int i = 0; i < 4; ++i) {
        f32x4 x = *(const f32x4*)(xch + (i * 64 + lane) * 4);
#pragma unroll
        for (int j = 0; j < 4; ++j) acc0[4 * i + j] += x[j];
      }
    }
    const float dval = c ? dval1 : dval0;
    const int A = b + 32 * hf;
    u16* orow = y2T + (size_t)(d0 + c) * 8 + 4 * h;
#pragma unroll
    for (int i = 0; i < 4; ++i) {
      u32x2 kvp = *(const u32x2*)(smem + K1_UOFF + c * UCH + (32 + A) * UBS + (8 * i + 4 * h) * 2);
      float kv[4] = {bflo(kvp[0]), bfhi(kvp[0]), bflo(kvp[1]), bfhi(kvp[1])};
      float o[4];
#pragma unroll
      for (int j = 0; j < 4; ++j) o[j] = rbf(acc0[4 * i + j]) + rbf(kv[j] * dval);
      u32x2 pk = {pack2(o[0], o[1]), pack2(o[2], o[3])};
      *(u32x2*)(orow + (size_t)(4 * A + i) * (D_ * 8)) = pk;
    }
  }
  STAMPW(1, 4);
}

template <int CTRL>
__device__ __forceinline__ float dpp_add(float x) {
  int y = __builtin_amdgcn_update_dpp(0, __float_as_int(x), CTRL, 0xf, 0xf, true);
  return x + __int_as_float(y);
}
__device__ __forceinline__ float wave_sum(float x) {
  x = dpp_add<0xB1>(x);
  x = dpp_add<0x4E>(x);
  x = dpp_add<0x141>(x);
  x = dpp_add<0x140>(x);
  int xi = __float_as_int(x);
  float a = __int_as_float(__builtin_amdgcn_readlane(xi, 0));
  float b = __int_as_float(__builtin_amdgcn_readlane(xi, 16));
  float c = __int_as_float(__builtin_amdgcn_readlane(xi, 32));
  float d = __int_as_float(__builtin_amdgcn_readlane(xi, 48));
  return (a + b) + (c + d);
}

__device__ __forceinline__ void ln_bf16x2(const float (&x)[2][8], float (&o)[2][8]) {
  float s[2], ss[2], mean[2];
#pragma unroll
  for (int r = 0; r < 2; ++r) {
    s[r] = ((x[r][0] + x[r][1]) + (x[r][2] + x[r][3])) + ((x[r][4] + x[r][5]) + (x[r][6] + x[r][7]));
  }
#pragma unroll
  for (int r = 0; r < 2; ++r) mean[r] = wave_sum(s[r]) * (1.f / 512.f);
#pragma unroll
  for (int r = 0; r < 2; ++r) {
    ss[r] = 0.f;
#pragma unroll
    for (int i = 0; i < 8; ++i) { float dlt = x[r][i] - mean[r]; ss[r] += dlt * dlt; }
  }
#pragma unroll
  for (int r = 0; r < 2; ++r) {
    const float var = wave_sum(ss[r]) * (1.f / 512.f);
    const float mb = rbf(mean[r]);
    const float vb = rbf(var);
    const float sd = rbf(sqrtf(rbf(vb + 1e-5f)));
    const float rc = 1.0f / sd;
#pragma unroll
    for (int i = 0; i < 8; ++i) {
      float a = rbf(x[r][i] - mb);
      float qt = a * rc;
      float rem = __builtin_fmaf(-qt, sd, a);
      qt = __builtin_fmaf(rem, rc, qt);
      o[r][i] = rbf(qt);
    }
  }
}

__global__ __launch_bounds__(256) void k1b_rows(const u16* __restrict__ q, const u16* __restrict__ y2T,
                                                u16* __restrict__ hout, u16* __restrict__ hln_f) {
  __shared__ __attribute__((aligned(16))) u32 tile[8][264];
  const int tid = threadIdx.x;
  const int s0 = blockIdx.x * 8;
  const int wave = tid >> 6, lane = tid & 63;
  STAMPW(2, 0);
  u32x4 ya = *(const u32x4*)(y2T + (size_t)blockIdx.x * (D_ * 8) + (2 * tid) * 8);
  u32x4 yb = *(const u32x4*)(y2T + (size_t)blockIdx.x * (D_ * 8) + (2 * tid + 1) * 8);
  u32x4 qv[2];
#pragma unroll
  for (int r = 0; r < 2; ++r) qv[r] = *(const u32x4*)(q + (size_t)(s0 + 2 * wave + r) * D_ + lane * 8);
  STAMPW(2, 1);
#pragma unroll
  for (int i = 0; i < 4; ++i) {
    tile[2 * i][tid] = (ya[i] & 0xffffu) | (yb[i] << 16);
    tile[2 * i + 1][tid] = (ya[i] >> 16) | (yb[i] & 0xffff0000u);
  }
  float x[2][8], ql[2][8];
#pragma unroll
  for (int r = 0; r < 2; ++r)
#pragma unroll
    for (int i = 0; i < 4; ++i) {
      x[r][2 * i] = bflo(qv[r][i]);
      x[r][2 * i + 1] = bfhi(qv[r][i]);
    }
  ln_bf16x2(x, ql);
  __syncthreads();
  float hh[2][8], hl[2][8];
#pragma unroll
  for (int r = 0; r < 2; ++r) {
    u32x4 yv = *(const u32x4*)(&tile[2 * wave + r][lane * 4]);
#pragma unroll
    for (int i = 0; i < 4; ++i) {
      hh[r][2 * i] = rbf(x[r][2 * i] + rbf(ql[r][2 * i] * bflo(yv[i])));
      hh[r][2 * i + 1] = rbf(x[r][2 * i + 1] + rbf(ql[r][2 * i + 1] * bfhi(yv[i])));
    }
  }
  ln_bf16x2(hh, hl);
#pragma unroll
  for (int r = 0; r < 2; ++r) {
    const int s = s0 + 2 * wave + r;
    u32x4 hp = {pack2(hh[r][0], hh[r][1]), pack2(hh[r][2], hh[r][3]), pack2(hh[r][4], hh[r][5]),
                pack2(hh[r][6], hh[r][7])};
    u32x4 lp = {pack2(hl[r][0], hl[r][1]), pack2(hl[r][2], hl[r][3]), pack2(hl[r][4], hl[r][5]),
                pack2(hl[r][6], hl[r][7])};
    *(u32x4*)(hout + (size_t)s * D_ + lane * 8) = hp;
    char* dst = (char*)hln_f + (size_t)(((s >> 7) * 16 + (lane >> 2)) * 8 + ((s >> 4) & 7)) * 1024 +
                ((lane & 3) * 16 + (s & 15)) * 16;
    *(u32x4*)dst = lp;
  }
  STAMPW(2, 2);
}

#define SGB(mask, n) __builtin_amdgcn_sched_group_barrier(mask, n, 0)
__global__ __launch_bounds__(256) void k2_gemm1(const u16* __restrict__ hln_f, const u16* __restrict__ Wgu_f,
                                                u16* __restrict__ hh_f) {
  extern __shared__ __attribute__((aligned(16))) char smem[];
  const int tid = threadIdx.x;
  const int wave = tid >> 6, lane = tid & 63;
  const int bid = blockIdx.x;
  const int xcd = bid & 7, idx = bid >> 3;
  const int ft = (xcd & 1) * 7 + (idx % 7);
  const int rb = (xcd >> 1) * 4 + (idx / 7);

  STAMPW(3, 0);
  const char* asrc = (const char*)hln_f + (size_t)rb * 131072 + tid * 16;
  u32x4 ast[2][8];
#pragma unroll
  for (int i = 0; i < 8; ++i) ast[0][i] = *(const u32x4*)(asrc + i * 4096);
  const char* wbase = (const char*)Wgu_f + (size_t)((ft * 8 + wave * 2) * 2) * 16 * 1024 + lane * 16;
  constexpr int PF = 4;
  bf16x8 wf[PF + 1][4];
#pragma unroll
  for (int p = 0; p < PF; ++p)
#pragma unroll
    for (int j = 0; j < 4; ++j) wf[p][j] = *(const bf16x8*)(wbase + (j * 16 + p) * 1024);
#pragma unroll
  for (int i = 0; i < 8; ++i) ast[1][i] = *(const u32x4*)(asrc + 32768 + i * 4096);

  f32x4 acc[4][8];
#pragma unroll
  for (int j = 0; j < 4; ++j)
#pragma unroll
    for (int m = 0; m < 8; ++m) acc[j][m] = f32x4{0.f, 0.f, 0.f, 0.f};

  bf16x8 xf[2][8];
#pragma unroll
  for (int ch = 0; ch < 4; ++ch) {
    __builtin_amdgcn_sched_barrier(0);
#pragma unroll
    for (int i = 0; i < 8; ++i) *(u32x4*)(smem + ch * 32768 + i * 4096 + tid * 16) = ast[ch & 1][i];
    if (ch + 2 < 4) {
#pragma unroll
      for (int i = 0; i < 8; ++i) ast[ch & 1][i] = *(const u32x4*)(asrc + (ch + 2) * 32768 + i * 4096);
    }
    __syncthreads();
    if (ch == 0) STAMPW(3, 1);
#pragma unroll
    for (int m = 0; m < 8; ++m) xf[0][m] = *(const bf16x8*)(smem + (ch * 32 + m) * 1024 + lane * 16);
    __builtin_amdgcn_sched_barrier(0);
#pragma unroll
    for (int k4 = 0; k4 < 4; ++k4) {
      const int kb = ch * 4 + k4;
      if (kb + PF < 16) {
#pragma unroll
        for (int j = 0; j < 4; ++j)
          wf[(kb + PF) % (PF + 1)][j] = *(const bf16x8*)(wbase + (j * 16 + kb + PF) * 1024);
      }
      if (k4 + 1 < 4) {
#pragma unroll
        for (int m = 0; m < 8; ++m)
          xf[(k4 + 1) & 1][m] = *(const bf16x8*)(smem + ((kb + 1) * 8 + m) * 1024 + lane * 16);
      }
#pragma unroll
      for (int j = 0; j < 4; ++j)
#pragma unroll
        for (int m = 0; m < 8; ++m)
          acc[j][m] = __builtin_amdgcn_mfma_f32_16x16x32_bf16(wf[kb % (PF + 1)][j], xf[k4 & 1][m], acc[j][m], 0, 0, 0);
#pragma unroll
      for (int g = 0; g < 4; ++g) {
        SGB(0x8, 4); SGB(0x100, 1); SGB(0x8, 4); SGB(0x100, 1); SGB(0x20, 1);
      }
      __builtin_amdgcn_sched_barrier(0);
    }
  }
  STAMPW(3, 2);
  const int qd = lane >> 4, sl = lane & 15;
  char* obase = (char*)hh_f + (size_t)(ft * 4 + wave) * 1024 + ((qd >> 1) * 16 + sl) * 16 + (qd & 1) * 8;
#pragma unroll
  for (int fbl = 0; fbl < 2; ++fbl)
#pragma unroll
    for (int m = 0; m < 8; ++m) {
      float o[4];
#pragma unroll
      for (int r = 0; r < 4; ++r) {
        float gt = rbf(acc[fbl * 2][m][r]);
        float up = rbf(acc[fbl * 2 + 1][m][r]);
        gt = gt * (gt > 0.f ? 1.f : 0.f);
        o[r] = gt * up;
      }
      u32x2 pk = {pack2(o[0], o[1]), pack2(o[2], o[3])};
      *(u32x2*)(obase + (size_t)(rb * 8 + m) * 56 * 1024 + fbl * 512) = pk;
    }
  STAMPW(3, 3);
}

__global__ __launch_bounds__(256) void k3_gemm2(const u16* __restrict__ hh_f, const u16* __restrict__ Wd_f,
                                                const u16* __restrict__ hin, u16* __restrict__ out) {
  __shared__ __attribute__((aligned(16))) char red[4 * 16 * 1024];
  const int tid = threadIdx.x;
  const int wave = tid >> 6, lane = tid & 63;
  const int bid = blockIdx.x;
  const int xcd = bid & 7, idx = bid >> 3;
  const int dt = idx & 7, st = xcd * 4 + (idx >> 3);

  STAMPW(4, 0);
  const char* wb = (const char*)Wd_f + (size_t)(dt * 4 * 56 + wave * 14) * 1024 + lane * 16;
  const char* xb = (const char*)hh_f + (size_t)(st * 4 * 56 + wave * 14) * 1024 + lane * 16;
  constexpr int PF = 5;
  bf16x8 wf[PF + 1][4], xf[PF + 1][4];
#pragma unroll
  for (int p = 0; p < PF; ++p)
#pragma unroll
    for (int j = 0; j < 4; ++j) {
      wf[p][j] = *(const bf16x8*)(wb + (j * 56 + p) * 1024);
      xf[p][j] = *(const bf16x8*)(xb + (j * 56 + p) * 1024);
    }
  const int qd = lane >> 4, sl = lane & 15;
  u32x2 hv[4];
#pragma unroll
  for (int j = 0; j < 4; ++j)
    hv[j] = *(const u32x2*)(hin + (size_t)(st * 64 + j * 16 + sl) * D_ + dt * 64 + wave * 16 + qd * 4);
  f32x4 acc[4][4];
#pragma unroll
  for (int i = 0; i < 4; ++i)
#pragma unroll
    for (int j = 0; j < 4; ++j) acc[i][j] = f32x4{0.f, 0.f, 0.f, 0.f};
  __builtin_amdgcn_sched_barrier(0);
#pragma unroll
  for (int kb = 0; kb < 14; ++kb) {
    if (kb + PF < 14) {
#pragma unroll
      for (int j = 0; j < 4; ++j) {
        wf[(kb + PF) % (PF + 1)][j] = *(const bf16x8*)(wb + (j * 56 + kb + PF) * 1024);
        xf[(kb + PF) % (PF + 1)][j] = *(const bf16x8*)(xb + (j * 56 + kb + PF) * 1024);
      }
    }
#pragma unroll
    for (int i = 0; i < 4; ++i)
#pragma unroll
      for (int j = 0; j < 4; ++j)
        acc[i][j] = __builtin_amdgcn_mfma_f32_16x16x32_bf16(wf[kb % (PF + 1)][i], xf[kb % (PF + 1)][j], acc[i][j], 0, 0, 0);
#pragma unroll
    for (int g = 0; g < 8; ++g) { SGB(0x8, 2); SGB(0x20, 1); }
    __builtin_amdgcn_sched_barrier(0);
  }
  STAMPW(4, 1);
#pragma unroll
  for (int i = 0; i < 4; ++i)
#pragma unroll
    for (int j = 0; j < 4; ++j)
      *(f32x4*)(red + ((wave * 4 + i) * 4 + j) * 1024 + lane * 16) = acc[i][j];
  __syncthreads();
#pragma unroll
  for (int j = 0; j < 4; ++j) {
    f32x4 sum = f32x4{0.f, 0.f, 0.f, 0.f};
#pragma unroll
    for (int w = 0; w < 4; ++w) sum += *(const f32x4*)(red + ((w * 4 + wave) * 4 + j) * 1024 + lane * 16);
    float hx[4] = {bflo(hv[j][0]), bfhi(hv[j][0]), bflo(hv[j][1]), bfhi(hv[j][1])};
    float o[4];
#pragma unroll
    for (int r = 0; r < 4; ++r) o[r] = hx[r] + rbf(sum[r]);
    u32x2 pk = {pack2(o[0], o[1]), pack2(o[2], o[3])};
    *(u32x2*)(out + (size_t)(st * 64 + j * 16 + sl) * D_ + dt * 64 + wave * 16 + qd * 4) = pk;
  }
  STAMPW(4, 2);
}

extern "C" void kernel_launch(void* const* d_in, const int* in_sizes, int n_in, void* d_out, int out_size,
                              void* d_ws, size_t ws_size, hipStream_t stream) {
  const u16* q = (const u16*)d_in[0];
  const u16* k = (const u16*)d_in[1];
  const u16* v = (const u16*)d_in[2];
  const u16* shiftk = (const u16*)d_in[3];
  const u16* ssmk = (const u16*)d_in[4];
  const u16* ssmD = (const u16*)d_in[5];
  const u16* Wg = (const u16*)d_in[7];
  const u16* Wu = (const u16*)d_in[8];
  const u16* Wd = (const u16*)d_in[9];
  char* ws = (char*)d_ws;
  u16* y2T = (u16*)(ws);
  u16* hbuf = (u16*)(ws + (2u << 20));
  u16* hln_f = (u16*)(ws + (4u << 20));
  u16* Wgu_f = (u16*)(ws + (6u << 20));
  u16* Wd_f = (u16*)(ws + (10u << 20));
  u16* hh_f = (u16*)(ws + (12u << 20));
  u16* kvT = (u16*)(ws + (20u << 20));

  (void)hipFuncSetAttribute((const void*)k1_conv, hipFuncAttributeMaxDynamicSharedMemorySize, K1_LDS);
  (void)hipFuncSetAttribute((const void*)k2_gemm1, hipFuncAttributeMaxDynamicSharedMemorySize, 131072);

  k0_pre<<<256, 256, 0, stream>>>(k, v, shiftk, kvT);
  k1_conv<<<256, 448, K1_LDS, stream>>>(kvT, ssmk, ssmD, y2T, Wg, Wu, Wd, Wgu_f, Wd_f);
  k1b_rows<<<256, 256, 0, stream>>>(q, y2T, hbuf, hln_f);
  k2_gemm1<<<224, 256, 131072, stream>>>(hln_f, Wgu_f, hh_f);
  k3_gemm2<<<256, 256, 0, stream>>>(hh_f, Wd_f, hbuf, (u16*)d_out);
}
```

```cpp
#include <hip/hip_runtime.h>
#include <hip/hip_bf16.h>

typedef unsigned short u16;
typedef unsigned int u32;
using bf16x8 = __attribute__((ext_vector_type(8))) __bf16;
using f32x4 = __attribute__((ext_vector_type(4))) float;
using f32x16 = __attribute__((ext_vector_type(16))) float;
using u32x4 = __attribute__((ext_vector_type(4))) u32;
using u32x2 = __attribute__((ext_vector_type(2))) u32;
using s16x4 = __attribute__((ext_vector_type(4))) short;
#define LDS_PTR __attribute__((address_space(3)))

constexpr int S_ = 2048, D_ = 512, F_ = 1792;

#define STAMPW(kid, idx)

__device__ __forceinline__ float bf2f(u32 u) { return __uint_as_float(u << 16); }
__device__ __forceinline__ float bflo(u32 u) { return __uint_as_float(u << 16); }
__device__ __forceinline__ float bfhi(u32 u) { return __uint_as_float(u & 0xffff0000u); }
__device__ __forceinline__ u32 f2bf(float f) {
  __bf16 b = (__bf16)f;
  return (u32)__builtin_bit_cast(u16, b);
}
__device__ __forceinline__ float rbf(float f) { return bf2f(f2bf(f)); }
__device__ __forceinline__ u32 pack2(float lo, float hi) { return f2bf(lo) | (f2bf(hi) << 16); }

__global__ __launch_bounds__(256) void k0_pre(const u16* __restrict__ kin, const u16* __restrict__ vin,
                                              const u16* __restrict__ shiftk, u16* __restrict__ kvT) {
  const int tid = threadIdx.x;
  const int bid = blockIdx.x;
  STAMPW(0, 0);
  const int ct = bid & 7, rt = bid >> 3;
  const int d0 = ct * 64 + (tid & 31) * 2;
  const int r0 = rt * 64 + (tid >> 5) * 8;
  u32 kw[11], vw[8];
  {
    const u32* kp = (const u32*)(kin + d0);
    const u32* vp = (const u32*)(vin + d0);
#pragma unroll
    for (int i = 0; i < 11; ++i) {
      int r = r0 - 3 + i;
      kw[i] = (r >= 0) ? kp[(size_t)r * (D_ / 2)] : 0u;
    }
#pragma unroll
    for (int i = 0; i < 8; ++i) vw[i] = vp[(size_t)(r0 + i) * (D_ / 2)];
  }
  const u32x2 sw0 = *(const u32x2*)(shiftk + (size_t)d0 * 4);
  const u32x2 sw1 = *(const u32x2*)(shiftk + (size_t)(d0 + 1) * 4);
  {
    float w[2][4] = {{bflo(sw0[0]), bfhi(sw0[0]), bflo(sw0[1]), bfhi(sw0[1])},
                     {bflo(sw1[0]), bfhi(sw1[0]), bflo(sw1[1]), bfhi(sw1[1])}};
#pragma unroll
    for (int c = 0; c < 2; ++c) {
      float kf[11];
#pragma unroll
      for (int i = 0; i < 11; ++i) kf[i] = c ? bfhi(kw[i]) : bflo(kw[i]);
      float u[8];
#pragma unroll
      for (int i = 0; i < 8; ++i) {
        float acc = rbf(kf[i] * w[c][3]);
        acc = rbf(acc + rbf(kf[i + 1] * w[c][2]));
        acc = rbf(acc + rbf(kf[i + 2] * w[c][1]));
        acc = rbf(acc + rbf(kf[i + 3] * w[c][0]));
        float vf = c ? bfhi(vw[i]) : bflo(vw[i]);
        u[i] = acc * vf;
      }
      u32x4 pk = {pack2(u[0], u[1]), pack2(u[2], u[3]), pack2(u[4], u[5]), pack2(u[6], u[7])};
      *(u32x4*)(kvT + (size_t)(d0 + c) * S_ + r0) = pk;
    }
  }
  STAMPW(0, 1);
}

constexpr int KCS = 4160;
constexpr int KCH = 8 * KCS;
constexpr int UBS = 80;
constexpr int UCH = 96 * UBS;
constexpr int K1_KOFF = 0;
constexpr int K1_UOFF = 2 * KCH;
constexpr int K1_ROFF = K1_UOFF + 2 * UCH;
constexpr int K1_WOFF = K1_ROFF + 2 * 4096;
constexpr int K1_LDS = K1_WOFF + 3 * 32 * 272;

__device__ __forceinline__ u32 rot16(u32 x) { return (x >> 16) | (x << 16); }

__device__ __forceinline__ constexpr int k1_boff(int j) {
  return ((((16 - 16 * j) + 2048) >> 5) - 64 + 32) * UBS + (((16 - 16 * j) + 2048) & 31) * 2;
}

__global__ __launch_bounds__(448) void k1_conv(const u16* __restrict__ kvT, const u16* __restrict__ ssmk,
                                               const u16* __restrict__ ssmD, u16* __restrict__ y2T,
                                               const u16* __restrict__ Wg, const u16* __restrict__ Wu,
                                               const u16* __restrict__ Wd, u16* __restrict__ Wgu_f,
                                               u16* __restrict__ Wd_f) {
  extern __shared__ __attribute__((aligned(16))) char smem[];
  const int tid = threadIdx.x;
  const int d0 = 2 * blockIdx.x;
  const int bid = blockIdx.x;
  const int wave = __builtin_amdgcn_readfirstlane(tid >> 6), lane = tid & 63;
  STAMPW(1, 0);
  if (wave >= 4) {
    const int wtask = bid * 3 + (wave - 4);
    __builtin_amdgcn_s_barrier();
    u32x4 rws[8];
    char* wdst = nullptr;
    int wstride = 0;
    if (wtask < 672) {
      const int which = wtask / 224, rem = wtask - which * 224;
      const u16* src;
      int ld, kb, nt;
      if (which < 2) {
        kb = rem / 14; nt = rem - kb * 14; ld = F_;
        src = (which == 0 ? Wg : Wu);
        wdst = (char*)Wgu_f + (size_t)((nt * 16 + which) * 16 + kb) * 1024;
        wstride = 32 * 1024;
      } else {
        kb = rem >> 2; nt = rem & 3; ld = D_;
        src = Wd;
        wdst = (char*)Wd_f + (size_t)(nt * 8 * 56 + kb) * 1024;
        wstride = 56 * 1024;
      }
      const u16* sp = src + (size_t)(kb * 32 + (lane >> 4)) * ld + nt * 128 + (lane & 15) * 8;
#pragma unroll
      for (int i = 0; i < 8; ++i) rws[i] = *(const u32x4*)(sp + (size_t)(4 * i) * ld);
    }
    if (wtask < 672) {
      char* wl = smem + K1_WOFF + (wave - 4) * (32 * 272);
#pragma unroll
      for (int i = 0; i < 8; ++i) *(u32x4*)(wl + (4 * i + (lane >> 4)) * 272 + (lane & 15) * 16) = rws[i];
      const char* rp = wl + (8 * (lane >> 4) + ((lane & 15) >> 2)) * 272 + (lane & 3) * 8;
#pragma unroll
      for (int j = 0; j < 8; ++j) {
        s16x4 lo = __builtin_amdgcn_ds_read_tr16_b64_v4i16((LDS_PTR s16x4*)(rp + j * 32));
        s16x4 hi = __builtin_amdgcn_ds_read_tr16_b64_v4i16((LDS_PTR s16x4*)(rp + j * 32 + 4 * 272));
        u32x2 l2 = __builtin_bit_cast(u32x2, lo), h2 = __builtin_bit_cast(u32x2, hi);
        *(u32x4*)(wdst + (size_t)j * wstride + lane * 16) = u32x4{l2[0], l2[1], h2[0], h2[1]};
      }
    }
    __builtin_amdgcn_s_barrier();
    return;
  }
  u32x4 g1[2], g0[2], uv[2];
#pragma unroll
  for (int c = 0; c < 2; ++c) {
    const u16* kr = ssmk + (size_t)(d0 + c) * S_;
    int a = 2040 - 8 * tid;
    g1[c] = *(const u32x4*)(kr + a);
    if (a >= 8) g0[c] = *(const u32x4*)(kr + a - 8);
    else g0[c] = u32x4{0, 0, 0, 0};
    uv[c] = *(const u32x4*)(kvT + (size_t)(d0 + c) * S_ + tid * 8);
  }
  const float dval0 = bf2f(ssmD[d0]), dval1 = bf2f(ssmD[d0 + 1]);
  {
    u32x4 z = {0, 0, 0, 0};
    for (int i = tid; i < 2 * 160; i += 256) {
      int c = i / 160, j = i - c * 160;
      *(u32x4*)(smem + K1_UOFF + c * UCH + j * 16) = z;
    }
    if (tid < 64) {
      int c = tid >> 5, r = (tid >> 2) & 7, q = 256 + (tid & 3);
      *(u32x4*)(smem + K1_KOFF + c * KCH + r * KCS + q * 16) = z;
    }
  }
  STAMPW(1, 1);
#pragma unroll
  for (int c = 0; c < 2; ++c) {
    u32 W[8], O[7];
#pragma unroll
    for (int i = 0; i < 4; ++i) {
      W[i] = rot16(g1[c][3 - i]);
      W[4 + i] = rot16(g0[c][3 - i]);
    }
#pragma unroll
    for (int i = 0; i < 7; ++i) O[i] = (W[i] >> 16) | (W[i + 1] << 16);
    char* base = smem + K1_KOFF + c * KCH + tid * 16;
#pragma unroll
    for (int r = 0; r < 8; ++r) {
      u32x4 val;
      if ((r & 1) == 0) val = u32x4{W[r / 2], W[r / 2 + 1], W[r / 2 + 2], W[r / 2 + 3]};
      else val = u32x4{O[r / 2], O[r / 2 + 1], O[r / 2 + 2], O[r / 2 + 3]};
      *(u32x4*)(base + r * KCS) = val;
    }
    *(u32x4*)(smem + K1_UOFF + c * UCH + (32 + (tid >> 2)) * UBS + (tid & 3) * 16) = uv[c];
  }
  __syncthreads();
  STAMPW(1, 2);

  const int c = wave >> 1, hf = wave & 1;
  const int b = lane & 31, h = lane >> 5;
  f32x16 acc0, acc1;
#pragma unroll
  for (int i = 0; i < 16; ++i) { acc0[i] = 0.f; acc1[i] = 0.f; }
  const int i0 = 2047 + 16 - b + 8 * h;
  const int r = i0 & 7;
  const char* abase = smem + K1_KOFF + c * KCH + r * KCS + (i0 - r) * 2;
  const char* ulo = smem + K1_UOFF + c * UCH + b * UBS + 16 * h;
  const char* uhi = ulo + 32 * UBS;
  constexpr int PD = 3;
  if (hf == 0) {
    const char* ap = abase - 32 * 63;
    bf16x8 af[PD + 1], bl[PD + 1], bh[PD + 1];
#pragma unroll
    for (int j = 0; j < PD; ++j) {
      af[j] = *(const bf16x8*)(ap + 32 * (63 - j));
      bl[j] = *(const bf16x8*)(ulo + k1_boff(j));
      bh[j] = *(const bf16x8*)(uhi + k1_boff(j));
    }
#pragma unroll
    for (int j = 0; j < 64; ++j) {
      if (j + PD < 64) {
        af[(j + PD) % (PD + 1)] = *(const bf16x8*)(ap + 32 * (63 - j - PD));
        bl[(j + PD) % (PD + 1)] = *(const bf16x8*)(ulo + k1_boff(j + PD));
        if (j + PD < 32) bh[(j + PD) % (PD + 1)] = *(const bf16x8*)(uhi + k1_boff(j + PD));
      }
      acc0 = __builtin_amdgcn_mfma_f32_32x32x16_bf16(af[j % (PD + 1)], bl[j % (PD + 1)], acc0, 0, 0, 0);
      if (j < 32) acc1 = __builtin_amdgcn_mfma_f32_32x32x16_bf16(af[j % (PD + 1)], bh[j % (PD + 1)], acc1, 0, 0, 0);
      __builtin_amdgcn_sched_group_barrier(0x8, 1, 0);
      __builtin_amdgcn_sched_group_barrier(0x100, 2, 0);
      __builtin_amdgcn_sched_group_barrier(0x8, 1, 0);
      __builtin_amdgcn_sched_group_barrier(0x100, 1, 0);
      __builtin_amdgcn_sched_barrier(0);
    }
  } else {
    const char* ap = abase - 32 * 127;
    bf16x8 af[PD + 1], bh[PD + 1];
#pragma unroll
    for (int j = 0; j < PD; ++j) {
      af[j] = *(const bf16x8*)(ap + 32 * (127 - 32 - j));
      bh[j] = *(const bf16x8*)(uhi + k1_boff(32 + j));
    }
#pragma unroll
    for (int j = 0; j < 96; ++j) {
      if (j + PD < 96) {
        af[(j + PD) % (PD + 1)] = *(const bf16x8*)(ap + 32 * (127 - 32 - j - PD));
        bh[(j + PD) % (PD + 1)] = *(const bf16x8*)(uhi + k1_boff(32 + j + PD));
      }
      acc0 = __builtin_amdgcn_mfma_f32_32x32x16_bf16(af[j % (PD + 1)], bh[j % (PD + 1)], acc0, 0, 0, 0);
      __builtin_amdgcn_sched_group_barrier(0x8, 1, 0);
      __builtin_amdgcn_sched_group_barrier(0x100, 2, 0);
      __builtin_amdgcn_sched_barrier(0);
    }
  }
  STAMPW(1, 3);
  {
    float* xch = (float*)(smem + K1_ROFF + c * 4096);
    if (hf == 0) {
#pragma unroll
      for (int i = 0; i < 4; ++i) {
        f32x4 x;
#pragma unroll
        for (int j = 0; j < 4; ++j) x[j] = acc1[4 * i + j];
        *(f32x4*)(xch + (i * 64 + lane) * 4) = x;
      }
    }
    __syncthreads();
    if (hf == 1) {
#pragma unroll
      for (int i = 0; i < 4; ++i) {
        f32x4 x = *(const f32x4*)(xch + (i * 64 + lane) * 4);
#pragma unroll
        for (int j = 0; j < 4; ++j) acc0[4 * i + j] += x[j];
      }
    }
    const float dval = c ? dval1 : dval0;
    const int A = b + 32 * hf;
    u16* orow = y2T + (size_t)(d0 + c) * 8 + 4 * h;
#pragma unroll
    for (int i = 0; i < 4; ++i) {
      u32x2 kvp = *(const u32x2*)(smem + K1_UOFF + c * UCH + (32 + A) * UBS + (8 * i + 4 * h) * 2);
      float kv[4] = {bflo(kvp[0]), bfhi(kvp[0]), bflo(kvp[1]), bfhi(kvp[1])};
      float o[4];
#pragma unroll
      for (int j = 0; j < 4; ++j) o[j] = rbf(acc0[4 * i + j]) + rbf(kv[j] * dval);
      u32x2 pk = {pack2(o[0], o[1]), pack2(o[2], o[3])};
      *(u32x2*)(orow + (size_t)(4 * A + i) * (D_ * 8)) = pk;
    }
  }
  STAMPW(1, 4);
}

template <int CTRL>
__device__ __forceinline__ float dpp_add(float x) {
  int y = __builtin_amdgcn_update_dpp(0, __float_as_int(x), CTRL, 0xf, 0xf, true);
  return x + __int_as_float(y);
}
__device__ __forceinline__ float wave_sum(float x) {
  x = dpp_add<0xB1>(x);
  x = dpp_add<0x4E>(x);
  x = dpp_add<0x141>(x);
  x = dpp_add<0x140>(x);
  int xi = __float_as_int(x);
  float a = __int_as_float(__builtin_amdgcn_readlane(xi, 0));
  float b = __int_as_float(__builtin_amdgcn_readlane(xi, 16));
  float c = __int_as_float(__builtin_amdgcn_readlane(xi, 32));
  float d = __int_as_float(__builtin_amdgcn_readlane(xi, 48));
  return (a + b) + (c + d);
}

__device__ __forceinline__ void ln_bf16x2(const float (&x)[2][8], float (&o)[2][8]) {
  float s[2], ss[2], mean[2];
#pragma unroll
  for (int r = 0; r < 2; ++r) {
    s[r] = ((x[r][0] + x[r][1]) + (x[r][2] + x[r][3])) + ((x[r][4] + x[r][5]) + (x[r][6] + x[r][7]));
  }
#pragma unroll
  for (int r = 0; r < 2; ++r) mean[r] = wave_sum(s[r]) * (1.f / 512.f);
#pragma unroll
  for (int r = 0; r < 2; ++r) {
    ss[r] = 0.f;
#pragma unroll
    for (int i = 0; i < 8; ++i) { float dlt = x[r][i] - mean[r]; ss[r] += dlt * dlt; }
  }
#pragma unroll
  for (int r = 0; r < 2; ++r) {
    const float var = wave_sum(ss[r]) * (1.f / 512.f);
    const float mb = rbf(mean[r]);
    const float vb = rbf(var);
    const float sd = rbf(sqrtf(rbf(vb + 1e-5f)));
    const float rc = 1.0f / sd;
#pragma unroll
    for (int i = 0; i < 8; ++i) {
      float a = rbf(x[r][i] - mb);
      float qt = a * rc;
      float rem = __builtin_fmaf(-qt, sd, a);
      qt = __builtin_fmaf(rem, rc, qt);
      o[r][i] = rbf(qt);
    }
  }
}

__global__ __launch_bounds__(256) void k1b_rows(const u16* __restrict__ q, const u16* __restrict__ y2T,
                                                u16* __restrict__ hout, u16* __restrict__ hln_f) {
  __shared__ __attribute__((aligned(16))) u32 tile[8][264];
  const int tid = threadIdx.x;
  const int s0 = blockIdx.x * 8;
  const int wave = tid >> 6, lane = tid & 63;
  STAMPW(2, 0);
  u32x4 ya = *(const u32x4*)(y2T + (size_t)blockIdx.x * (D_ * 8) + (2 * tid) * 8);
  u32x4 yb = *(const u32x4*)(y2T + (size_t)blockIdx.x * (D_ * 8) + (2 * tid + 1) * 8);
  u32x4 qv[2];
#pragma unroll
  for (int r = 0; r < 2; ++r) qv[r] = *(const u32x4*)(q + (size_t)(s0 + 2 * wave + r) * D_ + lane * 8);
  STAMPW(2, 1);
#pragma unroll
  for (int i = 0; i < 4; ++i) {
    tile[2 * i][tid] = (ya[i] & 0xffffu) | (yb[i] << 16);
    tile[2 * i + 1][tid] = (ya[i] >> 16) | (yb[i] & 0xffff0000u);
  }
  float x[2][8], ql[2][8];
#pragma unroll
  for (int r = 0; r < 2; ++r)
#pragma unroll
    for (int i = 0; i < 4; ++i) {
      x[r][2 * i] = bflo(qv[r][i]);
      x[r][2 * i + 1] = bfhi(qv[r][i]);
    }
  ln_bf16x2(x, ql);
  __syncthreads();
  float hh[2][8], hl[2][8];
#pragma unroll
  for (int r = 0; r < 2; ++r) {
    u32x4 yv = *(const u32x4*)(&tile[2 * wave + r][lane * 4]);
#pragma unroll
    for (int i = 0; i < 4; ++i) {
      hh[r][2 * i] = rbf(x[r][2 * i] + rbf(ql[r][2 * i] * bflo(yv[i])));
      hh[r][2 * i + 1] = rbf(x[r][2 * i + 1] + rbf(ql[r][2 * i + 1] * bfhi(yv[i])));
    }
  }
  ln_bf16x2(hh, hl);
#pragma unroll
  for (int r = 0; r < 2; ++r) {
    const int s = s0 + 2 * wave + r;
    u32x4 hp = {pack2(hh[r][0], hh[r][1]), pack2(hh[r][2], hh[r][3]), pack2(hh[r][4], hh[r][5]),
                pack2(hh[r][6], hh[r][7])};
    u32x4 lp = {pack2(hl[r][0], hl[r][1]), pack2(hl[r][2], hl[r][3]), pack2(hl[r][4], hl[r][5]),
                pack2(hl[r][6], hl[r][7])};
    *(u32x4*)(hout + (size_t)s * D_ + lane * 8) = hp;
    char* dst = (char*)hln_f + (size_t)(((s >> 7) * 16 + (lane >> 2)) * 8 + ((s >> 4) & 7)) * 1024 +
                ((lane & 3) * 16 + (s & 15)) * 16;
    *(u32x4*)dst = lp;
  }
  STAMPW(2, 2);
}

#define SGB(mask, n) __builtin_amdgcn_sched_group_barrier(mask, n, 0)
__global__ __launch_bounds__(512) void k2_gemm1(const u16* __restrict__ hln_f, const u16* __restrict__ Wgu_f,
                                                u16* __restrict__ hh_f) {
  extern __shared__ __attribute__((aligned(16))) char smem[];
  const int tid = threadIdx.x;
  const int wave = tid >> 6, lane = tid & 63;
  const int bid = blockIdx.x;
  const int xcd = bid & 7, idx = bid >> 3;
  const int ft = (xcd & 1) * 7 + (idx % 7);
  const int rb = (xcd >> 1) * 4 + (idx / 7);

  STAMPW(3, 0);
  const char* asrc = (const char*)hln_f + (size_t)rb * 131072 + tid * 16;
  u32x4 ast[2][4];
#pragma unroll
  for (int i = 0; i < 4; ++i) ast[0][i] = *(const u32x4*)(asrc + i * 8192);
  const char* wbase = (const char*)Wgu_f + (size_t)((ft * 8 + wave) * 2) * 16 * 1024 + lane * 16;
  constexpr int PF = 5;
  bf16x8 wf[PF + 1][2];
#pragma unroll
  for (int p = 0; p < PF; ++p)
#pragma unroll
    for (int j = 0; j < 2; ++j) wf[p][j] = *(const bf16x8*)(wbase + (j * 16 + p) * 1024);
#pragma unroll
  for (int i = 0; i < 4; ++i) ast[1][i] = *(const u32x4*)(asrc + 32768 + i * 8192);

  f32x4 acc[2][8];
#pragma unroll
  for (int j = 0; j < 2; ++j)
#pragma unroll
    for (int m = 0; m < 8; ++m) acc[j][m] = f32x4{0.f, 0.f, 0.f, 0.f};

  bf16x8 xf[2][8];
#pragma unroll
  for (int ch = 0; ch < 4; ++ch) {
    __builtin_amdgcn_sched_barrier(0);
#pragma unroll
    for (int i = 0; i < 4; ++i) *(u32x4*)(smem + ch * 32768 + i * 8192 + tid * 16) = ast[ch & 1][i];
    if (ch + 2 < 4) {
#pragma unroll
      for (int i = 0; i < 4; ++i) ast[ch & 1][i] = *(const u32x4*)(asrc + (ch + 2) * 32768 + i * 8192);
    }
    __syncthreads();
    if (ch == 0) STAMPW(3, 1);
#pragma unroll
    for (int m = 0; m < 8; ++m) xf[0][m] = *(const bf16x8*)(smem + (ch * 32 + m) * 1024 + lane * 16);
    __builtin_amdgcn_sched_barrier(0);
#pragma unroll
    for (int k4 = 0; k4 < 4; ++k4) {
      const int kb = ch * 4 + k4;
      if (kb + PF < 16) {
#pragma unroll
        for (int j = 0; j < 2; ++j)
          wf[(kb + PF) % (PF + 1)][j] = *(const bf16x8*)(wbase + (j * 16 + kb + PF) * 1024);
      }
      if (k4 + 1 < 4) {
#pragma unroll
        for (int m = 0; m < 8; ++m)
          xf[(k4 + 1) & 1][m] = *(const bf16x8*)(smem + ((kb + 1) * 8 + m) * 1024 + lane * 16);
      }
#pragma unroll
      for (int m = 0; m < 8; ++m)
#pragma unroll
        for (int j = 0; j < 2; ++j)
          acc[j][m] = __builtin_amdgcn_mfma_f32_16x16x32_bf16(wf[kb % (PF + 1)][j], xf[k4 & 1][m], acc[j][m], 0, 0, 0);
#pragma unroll
      for (int g = 0; g < 2; ++g) {
        SGB(0x8, 2); SGB(0x100, 1); SGB(0x8, 2); SGB(0x100, 1); SGB(0x8, 2); SGB(0x100, 1); SGB(0x8, 2); SGB(0x100, 1); SGB(0x20, 1);
      }
      __builtin_amdgcn_sched_barrier(0);
    }
  }
  STAMPW(3, 2);
  const int qd = lane >> 4, sl = lane & 15;
  char* obase = (char*)hh_f + (size_t)(ft * 4 + (wave >> 1)) * 1024 + (wave & 1) * 512 + ((qd >> 1) * 16 + sl) * 16 +
                (qd & 1) * 8;
#pragma unroll
  for (int m = 0; m < 8; ++m) {
    float o[4];
#pragma unroll
    for (int r = 0; r < 4; ++r) {
      float gt = rbf(acc[0][m][r]);
      float up = rbf(acc[1][m][r]);
      gt = gt * (gt > 0.f ? 1.f : 0.f);
      o[r] = gt * up;
    }
    u32x2 pk = {pack2(o[0], o[1]), pack2(o[2], o[3])};
    *(u32x2*)(obase + (size_t)(rb * 8 + m) * 56 * 1024) = pk;
  }
  STAMPW(3, 3);
}

__global__ __launch_bounds__(256) void k3_gemm2(const u16* __restrict__ hh_f, const u16* __restrict__ Wd_f,
                                                const u16* __restrict__ hin, u16* __restrict__ out) {
  __shared__ __attribute__((aligned(16))) char red[4 * 16 * 1024];
  const int tid = threadIdx.x;
  const int wave = tid >> 6, lane = tid & 63;
  const int bid = blockIdx.x;
  const int xcd = bid & 7, idx = bid >> 3;
  const int dt = idx & 7, st = xcd * 4 + (idx >> 3);

  STAMPW(4, 0);
  const char* wb = (const char*)Wd_f + (size_t)(dt * 4 * 56 + wave * 14) * 1024 + lane * 16;
  const char* xb = (const char*)hh_f + (size_t)(st * 4 * 56 + wave * 14) * 1024 + lane * 16;
  const int rot = 0;
  auto koff = [&](int kb) { int x = kb + rot; return (x >= 14 ? x - 14 : x) * 1024; };
  constexpr int PF = 5;
  bf16x8 wf[PF + 1][4], xf[PF + 1][4];
#pragma unroll
  for (int p = 0; p < PF; ++p)
#pragma unroll
    for (int j = 0; j < 4; ++j) {
      wf[p][j] = *(const bf16x8*)(wb + j * 56 * 1024 + koff(p));
      xf[p][j] = *(const bf16x8*)(xb + j * 56 * 1024 + koff(p));
    }
  const int qd = lane >> 4, sl = lane & 15;
  u32x2 hv[4];
#pragma unroll
  for (int j = 0; j < 4; ++j)
    hv[j] = *(const u32x2*)(hin + (size_t)(st * 64 + j * 16 + sl) * D_ + dt * 64 + wave * 16 + qd * 4);
  f32x4 acc[4][4];
#pragma unroll
  for (int i = 0; i < 4; ++i)
#pragma unroll
    for (int j = 0; j < 4; ++j) acc[i][j] = f32x4{0.f, 0.f, 0.f, 0.f};
  __builtin_amdgcn_sched_barrier(0);
#pragma unroll
  for (int kb = 0; kb < 14; ++kb) {
    if (kb + PF < 14) {
#pragma unroll
      for (int j = 0; j < 4; ++j) {
        wf[(kb + PF) % (PF + 1)][j] = *(const bf16x8*)(wb + j * 56 * 1024 + koff(kb + PF));
        xf[(kb + PF) % (PF + 1)][j] = *(const bf16x8*)(xb + j * 56 * 1024 + koff(kb + PF));
      }
    }
#pragma unroll
    for (int i = 0; i < 4; ++i)
#pragma unroll
      for (int j = 0; j < 4; ++j)
        acc[i][j] = __builtin_amdgcn_mfma_f32_16x16x32_bf16(wf[kb % (PF + 1)][i], xf[kb % (PF + 1)][j], acc[i][j], 0, 0, 0);
#pragma unroll
    for (int g = 0; g < 8; ++g) { SGB(0x8, 2); SGB(0x20, 1); }
    __builtin_amdgcn_sched_barrier(0);
  }
  STAMPW(4, 1);
#pragma unroll
  for (int i = 0; i < 4; ++i)
#pragma unroll
    for (int j = 0; j < 4; ++j)
      *(f32x4*)(red + ((wave * 4 + i) * 4 + j) * 1024 + lane * 16) = acc[i][j];
  __syncthreads();
#pragma unroll
  for (int j = 0; j < 4; ++j) {
    f32x4 sum = f32x4{0.f, 0.f, 0.f, 0.f};
#pragma unroll
    for (int w = 0; w < 4; ++w) sum += *(const f32x4*)(red + ((w * 4 + wave) * 4 + j) * 1024 + lane * 16);
    float hx[4] = {bflo(hv[j][0]), bfhi(hv[j][0]), bflo(hv[j][1]), bfhi(hv[j][1])};
    float o[4];
#pragma unroll
    for (int r = 0; r < 4; ++r) o[r] = hx[r] + rbf(sum[r]);
    u32x2 pk = {pack2(o[0], o[1]), pack2(o[2], o[3])};
    *(u32x2*)(out + (size_t)(st * 64 + j * 16 + sl) * D_ + dt * 64 + wave * 16 + qd * 4) = pk;
  }
  STAMPW(4, 2);
}

extern "C" void kernel_launch(void* const* d_in, const int* in_sizes, int n_in, void* d_out, int out_size,
                              void* d_ws, size_t ws_size, hipStream_t stream) {
  const u16* q = (const u16*)d_in[0];
  const u16* k = (const u16*)d_in[1];
  const u16* v = (const u16*)d_in[2];
  const u16* shiftk = (const u16*)d_in[3];
  const u16* ssmk = (const u16*)d_in[4];
  const u16* ssmD = (const u16*)d_in[5];
  const u16* Wg = (const u16*)d_in[7];
  const u16* Wu = (const u16*)d_in[8];
  const u16* Wd = (const u16*)d_in[9];
  char* ws = (char*)d_ws;
  u16* y2T = (u16*)(ws);
  u16* hbuf = (u16*)(ws + (2u << 20));
  u16* hln_f = (u16*)(ws + (4u << 20));
  u16* Wgu_f = (u16*)(ws + (6u << 20));
  u16* Wd_f = (u16*)(ws + (10u << 20));
  u16* hh_f = (u16*)(ws + (12u << 20));
  u16* kvT = (u16*)(ws + (20u << 20));

  (void)hipFuncSetAttribute((const void*)k1_conv, hipFuncAttributeMaxDynamicSharedMemorySize, K1_LDS);
  (void)hipFuncSetAttribute((const void*)k2_gemm1, hipFuncAttributeMaxDynamicSharedMemorySize, 131072);

  k0_pre<<<256, 256, 0, stream>>>(k, v, shiftk, kvT);
  k1_conv<<<256, 448, K1_LDS, stream>>>(kvT, ssmk, ssmD, y2T, Wg, Wu, Wd, Wgu_f, Wd_f);
  k1b_rows<<<256, 256, 0, stream>>>(q, y2T, hbuf, hln_f);
  k2_gemm1<<<224, 512, 131072, stream>>>(hln_f, Wgu_f, hh_f);
  k3_gemm2<<<256, 256, 0, stream>>>(hh_f, Wd_f, hbuf, (u16*)d_out);
}
```

```cpp
#include <hip/hip_runtime.h>
#include <hip/hip_bf16.h>

typedef unsigned short u16;
typedef unsigned int u32;
using bf16x8 = __attribute__((ext_vector_type(8))) __bf16;
using f32x4 = __attribute__((ext_vector_type(4))) float;
using f32x16 = __attribute__((ext_vector_type(16))) float;
using u32x4 = __attribute__((ext_vector_type(4))) u32;
using u32x2 = __attribute__((ext_vector_type(2))) u32;
using s16x4 = __attribute__((ext_vector_type(4))) short;
#define LDS_PTR __attribute__((address_space(3)))

constexpr int S_ = 2048, D_ = 512, F_ = 1792;

#define STAMPW(kid, idx)

__device__ __forceinline__ float bf2f(u32 u) { return __uint_as_float(u << 16); }
__device__ __forceinline__ float bflo(u32 u) { return __uint_as_float(u << 16); }
__device__ __forceinline__ float bfhi(u32 u) { return __uint_as_float(u & 0xffff0000u); }
__device__ __forceinline__ u32 f2bf(float f) {
  __bf16 b = (__bf16)f;
  return (u32)__builtin_bit_cast(u16, b);
}
__device__ __forceinline__ float rbf(float f) { return bf2f(f2bf(f)); }
__device__ __forceinline__ u32 pack2(float lo, float hi) { return f2bf(lo) | (f2bf(hi) << 16); }

__global__ __launch_bounds__(256) void k0_pre(const u16* __restrict__ kin, const u16* __restrict__ vin,
                                              const u16* __restrict__ shiftk, u16* __restrict__ kvT) {
  const int tid = threadIdx.x;
  const int bid = blockIdx.x;
  STAMPW(0, 0);
  const int ct = bid & 7, rt = bid >> 3;
  const int d0 = ct * 64 + (tid & 31) * 2;
  const int r0 = rt * 64 + (tid >> 5) * 8;
  u32 kw[11], vw[8];
  {
    const u32* kp = (const u32*)(kin + d0);
    const u32* vp = (const u32*)(vin + d0);
#pragma unroll
    for (int i = 0; i < 11; ++i) {
      int r = r0 - 3 + i;
      kw[i] = (r >= 0) ? kp[(size_t)r * (D_ / 2)] : 0u;
    }
#pragma unroll
    for (int i = 0; i < 8; ++i) vw[i] = vp[(size_t)(r0 + i) * (D_ / 2)];
  }
  const u32x2 sw0 = *(const u32x2*)(shiftk + (size_t)d0 * 4);
  const u32x2 sw1 = *(const u32x2*)(shiftk + (size_t)(d0 + 1) * 4);
  {
    float w[2][4] = {{bflo(sw0[0]), bfhi(sw0[0]), bflo(sw0[1]), bfhi(sw0[1])},
                     {bflo(sw1[0]), bfhi(sw1[0]), bflo(sw1[1]), bfhi(sw1[1])}};
#pragma unroll
    for (int c = 0; c < 2; ++c) {
      float kf[11];
#pragma unroll
      for (int i = 0; i < 11; ++i) kf[i] = c ? bfhi(kw[i]) : bflo(kw[i]);
      float u[8];
#pragma unroll
      for (int i = 0; i < 8; ++i) {
        float acc = rbf(kf[i] * w[c][3]);
        acc = rbf(acc + rbf(kf[i + 1] * w[c][2]));
        acc = rbf(acc + rbf(kf[i + 2] * w[c][1]));
        acc = rbf(acc + rbf(kf[i + 3] * w[c][0]));
        float vf = c ? bfhi(vw[i]) : bflo(vw[i]);
        u[i] = acc * vf;
      }
      u32x4 pk = {pack2(u[0], u[1]), pack2(u[2], u[3]), pack2(u[4], u[5]), pack2(u[6], u[7])};
      *(u32x4*)(kvT + (size_t)(d0 + c) * S_ + r0) = pk;
    }
  }
  STAMPW(0, 1);
}

constexpr int KCS = 4160;
constexpr int KCH = 8 * KCS;
constexpr int UBS = 80;
constexpr int UCH = 96 * UBS;
constexpr int K1_KOFF = 0;
constexpr int K1_UOFF = 2 * KCH;
constexpr int K1_ROFF = K1_UOFF + 2 * UCH;
constexpr int K1_WOFF = K1_ROFF + 2 * 4096;
constexpr int K1_LDS = K1_WOFF + 3 * 32 * 272;

__device__ __forceinline__ u32 rot16(u32 x) { return (x >> 16) | (x << 16); }

__device__ __forceinline__ constexpr int k1_boff(int j) {
  return ((((16 - 16 * j) + 2048) >> 5) - 64 + 32) * UBS + (((16 - 16 * j) + 2048) & 31) * 2;
}

__global__ __launch_bounds__(448) void k1_conv(const u16* __restrict__ kvT, const u16* __restrict__ ssmk,
                                               const u16* __restrict__ ssmD, u16* __restrict__ y2T,
                                               const u16* __restrict__ Wg, const u16* __restrict__ Wu,
                                               const u16* __restrict__ Wd, u16* __restrict__ Wgu_f,
                                               u16* __restrict__ Wd_f) {
  extern __shared__ __attribute__((aligned(16))) char smem[];
  const int tid = threadIdx.x;
  const int d0 = 2 * blockIdx.x;
  const int bid = blockIdx.x;
  const int wave = __builtin_amdgcn_readfirstlane(tid >> 6), lane = tid & 63;
  STAMPW(1, 0);
  if (wave >= 4) {
    const int wtask = bid * 3 + (wave - 4);
    __builtin_amdgcn_s_barrier();
    u32x4 rws[8];
    char* wdst = nullptr;
    int wstride = 0;
    if (wtask < 672) {
      const int which = wtask / 224, rem = wtask - which * 224;
      const u16* src;
      int ld, kb, nt;
      if (which < 2) {
        kb = rem / 14; nt = rem - kb * 14; ld = F_;
        src = (which == 0 ? Wg : Wu);
        wdst = (char*)Wgu_f + (size_t)((nt * 16 + which) * 16 + kb) * 1024;
        wstride = 32 * 1024;
      } else {
        kb = rem >> 2; nt = rem & 3; ld = D_;
        src = Wd;
        wdst = (char*)Wd_f + (size_t)(nt * 8 * 56 + kb) * 1024;
        wstride = 56 * 1024;
      }
      const u16* sp = src + (size_t)(kb * 32 + (lane >> 4)) * ld + nt * 128 + (lane & 15) * 8;
#pragma unroll
      for (int i = 0; i < 8; ++i) rws[i] = *(const u32x4*)(sp + (size_t)(4 * i) * ld);
    }
    if (wtask < 672) {
      char* wl = smem + K1_WOFF + (wave - 4) * (32 * 272);
#pragma unroll
      for (int i = 0; i < 8; ++i) *(u32x4*)(wl + (4 * i + (lane >> 4)) * 272 + (lane & 15) * 16) = rws[i];
      const char* rp = wl + (8 * (lane >> 4) + ((lane & 15) >> 2)) * 272 + (lane & 3) * 8;
#pragma unroll
      for (int j = 0; j < 8; ++j) {
        s16x4 lo = __builtin_amdgcn_ds_read_tr16_b64_v4i16((LDS_PTR s16x4*)(rp + j * 32));
        s16x4 hi = __builtin_amdgcn_ds_read_tr16_b64_v4i16((LDS_PTR s16x4*)(rp + j * 32 + 4 * 272));
        u32x2 l2 = __builtin_bit_cast(u32x2, lo), h2 = __builtin_bit_cast(u32x2, hi);
        *(u32x4*)(wdst + (size_t)j * wstride + lane * 16) = u32x4{l2[0], l2[1], h2[0], h2[1]};
      }
    }
    __builtin_amdgcn_s_barrier();
    return;
  }
  u32x4 g1[2], g0[2], uv[2];
#pragma unroll
  for (int c = 0; c < 2; ++c) {
    const u16* kr = ssmk + (size_t)(d0 + c) * S_;
    int a = 2040 - 8 * tid;
    g1[c] = *(const u32x4*)(kr + a);
    if (a >= 8) g0[c] = *(const u32x4*)(kr + a - 8);
    else g0[c] = u32x4{0, 0, 0, 0};
    uv[c] = *(const u32x4*)(kvT + (size_t)(d0 + c) * S_ + tid * 8);
  }
  const float dval0 = bf2f(ssmD[d0]), dval1 = bf2f(ssmD[d0 + 1]);
  {
    u32x4 z = {0, 0, 0, 0};
    for (int i = tid; i < 2 * 160; i += 256) {
      int c = i / 160, j = i - c * 160;
      *(u32x4*)(smem + K1_UOFF + c * UCH + j * 16) = z;
    }
    if (tid < 64) {
      int c = tid >> 5, r = (tid >> 2) & 7, q = 256 + (tid & 3);
      *(u32x4*)(smem + K1_KOFF + c * KCH + r * KCS + q * 16) = z;
    }
  }
  STAMPW(1, 1);
#pragma unroll
  for (int c = 0; c < 2; ++c) {
    u32 W[8], O[7];
#pragma unroll
    for (int i = 0; i < 4; ++i) {
      W[i] = rot16(g1[c][3 - i]);
      W[4 + i] = rot16(g0[c][3 - i]);
    }
#pragma unroll
    for (int i = 0; i < 7; ++i) O[i] = (W[i] >> 16) | (W[i + 1] << 16);
    char* base = smem + K1_KOFF + c * KCH + tid * 16;
#pragma unroll
    for (int r = 0; r < 8; ++r) {
      u32x4 val;
      if ((r & 1) == 0) val = u32x4{W[r / 2], W[r / 2 + 1], W[r / 2 + 2], W[r / 2 + 3]};
      else val = u32x4{O[r / 2], O[r / 2 + 1], O[r / 2 + 2], O[r / 2 + 3]};
      *(u32x4*)(base + r * KCS) = val;
    }
    *(u32x4*)(smem + K1_UOFF + c * UCH + (32 + (tid >> 2)) * UBS + (tid & 3) * 16) = uv[c];
  }
  __syncthreads();
  STAMPW(1, 2);

  const int c = wave >> 1, hf = wave & 1;
  const int b = lane & 31, h = lane >> 5;
  f32x16 acc0, acc1;
#pragma unroll
  for (int i = 0; i < 16; ++i) { acc0[i] = 0.f; acc1[i] = 0.f; }
  const int i0 = 2047 + 16 - b + 8 * h;
  const int r = i0 & 7;
  const char* abase = smem + K1_KOFF + c * KCH + r * KCS + (i0 - r) * 2;
  const char* ulo = smem + K1_UOFF + c * UCH + b * UBS + 16 * h;
  const char* uhi = ulo + 32 * UBS;
  constexpr int PD = 3;
  if (hf == 0) {
    const char* ap = abase - 32 * 63;
    bf16x8 af[PD + 1], bl[PD + 1], bh[PD + 1];
#pragma unroll
    for (int j = 0; j < PD; ++j) {
      af[j] = *(const bf16x8*)(ap + 32 * (63 - j));
      bl[j] = *(const bf16x8*)(ulo + k1_boff(j));
      bh[j] = *(const bf16x8*)(uhi + k1_boff(j));
    }
#pragma unroll
    for (int j = 0; j < 64; ++j) {
      if (j + PD < 64) {
        af[(j + PD) % (PD + 1)] = *(const bf16x8*)(ap + 32 * (63 - j - PD));
        bl[(j + PD) % (PD + 1)] = *(const bf16x8*)(ulo + k1_boff(j + PD));
        if (j + PD < 32) bh[(j + PD) % (PD + 1)] = *(const bf16x8*)(uhi + k1_boff(j + PD));
      }
      acc0 = __builtin_amdgcn_mfma_f32_32x32x16_bf16(af[j % (PD + 1)], bl[j % (PD + 1)], acc0, 0, 0, 0);
      if (j < 32) acc1 = __builtin_amdgcn_mfma_f32_32x32x16_bf16(af[j % (PD + 1)], bh[j % (PD + 1)], acc1, 0, 0, 0);
      __builtin_amdgcn_sched_group_barrier(0x8, 1, 0);
      __builtin_amdgcn_sched_group_barrier(0x100, 2, 0);
      __builtin_amdgcn_sched_group_barrier(0x8, 1, 0);
      __builtin_amdgcn_sched_group_barrier(0x100, 1, 0);
      __builtin_amdgcn_sched_barrier(0);
    }
  } else {
    const char* ap = abase - 32 * 127;
    bf16x8 af[PD + 1], bh[PD + 1];
#pragma unroll
    for (int j = 0; j < PD; ++j) {
      af[j] = *(const bf16x8*)(ap + 32 * (127 - 32 - j));
      bh[j] = *(const bf16x8*)(uhi + k1_boff(32 + j));
    }
#pragma unroll
    for (int j = 0; j < 96; ++j) {
      if (j + PD < 96) {
        af[(j + PD) % (PD + 1)] = *(const bf16x8*)(ap + 32 * (127 - 32 - j - PD));
        bh[(j + PD) % (PD + 1)] = *(const bf16x8*)(uhi + k1_boff(32 + j + PD));
      }
      acc0 = __builtin_amdgcn_mfma_f32_32x32x16_bf16(af[j % (PD + 1)], bh[j % (PD + 1)], acc0, 0, 0, 0);
      __builtin_amdgcn_sched_group_barrier(0x8, 1, 0);
      __builtin_amdgcn_sched_group_barrier(0x100, 2, 0);
      __builtin_amdgcn_sched_barrier(0);
    }
  }
  STAMPW(1, 3);
  {
    float* xch = (float*)(smem + K1_ROFF + c * 4096);
    if (hf == 0) {
#pragma unroll
      for (int i = 0; i < 4; ++i) {
        f32x4 x;
#pragma unroll
        for (int j = 0; j < 4; ++j) x[j] = acc1[4 * i + j];
        *(f32x4*)(xch + (i * 64 + lane) * 4) = x;
      }
    }
    __syncthreads();
    if (hf == 1) {
#pragma unroll
      for (int i = 0; i < 4; ++i) {
        f32x4 x = *(const f32x4*)(xch + (i * 64 + lane) * 4);
#pragma unroll
        for (int j = 0; j < 4; ++j) acc0[4 * i + j] += x[j];
      }
    }
    const float dval = c ? dval1 : dval0;
    const int A = b + 32 * hf;
    u16* orow = y2T + (size_t)(d0 + c) * 8 + 4 * h;
#pragma unroll
    for (int i = 0; i < 4; ++i) {
      u32x2 kvp = *(const u32x2*)(smem + K1_UOFF + c * UCH + (32 + A) * UBS + (8 * i + 4 * h) * 2);
      float kv[4] = {bflo(kvp[0]), bfhi(kvp[0]), bflo(kvp[1]), bfhi(kvp[1])};
      float o[4];
#pragma unroll
      for (int j = 0; j < 4; ++j) o[j] = rbf(acc0[4 * i + j]) + rbf(kv[j] * dval);
      u32x2 pk = {pack2(o[0], o[1]), pack2(o[2], o[3])};
      *(u32x2*)(orow + (size_t)(4 * A + i) * (D_ * 8)) = pk;
    }
  }
  STAMPW(1, 4);
}

template <int CTRL>
__device__ __forceinline__ float dpp_add(float x) {
  int y = __builtin_amdgcn_update_dpp(0, __float_as_int(x), CTRL, 0xf, 0xf, true);
  return x + __int_as_float(y);
}
__device__ __forceinline__ float wave_sum(float x) {
  x = dpp_add<0xB1>(x);
  x = dpp_add<0x4E>(x);
  x = dpp_add<0x141>(x);
  x = dpp_add<0x140>(x);
  int xi = __float_as_int(x);
  float a = __int_as_float(__builtin_amdgcn_readlane(xi, 0));
  float b = __int_as_float(__builtin_amdgcn_readlane(xi, 16));
  float c = __int_as_float(__builtin_amdgcn_readlane(xi, 32));
  float d = __int_as_float(__builtin_amdgcn_readlane(xi, 48));
  return (a + b) + (c + d);
}

__device__ __forceinline__ void ln_bf16(const float (&x)[8], float (&o)[8]) {
  float s = ((x[0] + x[1]) + (x[2] + x[3])) + ((x[4] + x[5]) + (x[6] + x[7]));
  const float mean = wave_sum(s) * (1.f / 512.f);
  float ss = 0.f;
#pragma unroll
  for (int i = 0; i < 8; ++i) { float dlt = x[i] - mean; ss += dlt * dlt; }
  const float var = wave_sum(ss) * (1.f / 512.f);
  const float mb = rbf(mean);
  const float vb = rbf(var);
  const float sd = rbf(sqrtf(rbf(vb + 1e-5f)));
  const float rc = 1.0f / sd;
#pragma unroll
  for (int i = 0; i < 8; ++i) {
    float a = rbf(x[i] - mb);
    float qt = a * rc;
    float rem = __builtin_fmaf(-qt, sd, a);
    qt = __builtin_fmaf(rem, rc, qt);
    o[i] = rbf(qt);
  }
}

__global__ __launch_bounds__(512) void k1b_rows(const u16* __restrict__ q, const u16* __restrict__ y2T,
                                                u16* __restrict__ hout, u16* __restrict__ hln_f) {
  __shared__ __attribute__((aligned(16))) u16 tile[8][528];
  const int tid = threadIdx.x;
  const int s0 = blockIdx.x * 8;
  const int wave = tid >> 6, lane = tid & 63;
  STAMPW(2, 0);
  u32x4 ya = *(const u32x4*)(y2T + (size_t)blockIdx.x * (D_ * 8) + tid * 8);
  u32x4 qv = *(const u32x4*)(q + (size_t)(s0 + wave) * D_ + lane * 8);
  STAMPW(2, 1);
#pragma unroll
  for (int i = 0; i < 4; ++i) {
    tile[2 * i][tid] = (u16)(ya[i] & 0xffffu);
    tile[2 * i + 1][tid] = (u16)(ya[i] >> 16);
  }
  float x[8], ql[8];
#pragma unroll
  for (int i = 0; i < 4; ++i) {
    x[2 * i] = bflo(qv[i]);
    x[2 * i + 1] = bfhi(qv[i]);
  }
  ln_bf16(x, ql);
  __syncthreads();
  float hh[8], hl[8];
  {
    u32x4 yv = *(const u32x4*)(&tile[wave][lane * 8]);
#pragma unroll
    for (int i = 0; i < 4; ++i) {
      hh[2 * i] = rbf(x[2 * i] + rbf(ql[2 * i] * bflo(yv[i])));
      hh[2 * i + 1] = rbf(x[2 * i + 1] + rbf(ql[2 * i + 1] * bfhi(yv[i])));
    }
  }
  const int s = s0 + wave;
  u32x4 hp = {pack2(hh[0], hh[1]), pack2(hh[2], hh[3]), pack2(hh[4], hh[5]), pack2(hh[6], hh[7])};
  *(u32x4*)(hout + (size_t)s * D_ + lane * 8) = hp;
  ln_bf16(hh, hl);
  {
    u32x4 lp = {pack2(hl[0], hl[1]), pack2(hl[2], hl[3]), pack2(hl[4], hl[5]), pack2(hl[6], hl[7])};
    char* dst = (char*)hln_f + (size_t)(((s >> 7) * 16 + (lane >> 2)) * 8 + ((s >> 4) & 7)) * 1024 +
                ((lane & 3) * 16 + (s & 15)) * 16;
    *(u32x4*)dst = lp;
  }
  STAMPW(2, 2);
}

#define SGB(mask, n) __builtin_amdgcn_sched_group_barrier(mask, n, 0)
__global__ __launch_bounds__(512) void k2_gemm1(const u16* __restrict__ hln_f, const u16* __restrict__ Wgu_f,
                                                u16* __restrict__ hh_f) {
  extern __shared__ __attribute__((aligned(16))) char smem[];
  const int tid = threadIdx.x;
  const int wave = tid >> 6, lane = tid & 63;
  const int bid = blockIdx.x;
  const int xcd = bid & 7, idx = bid >> 3;
  const int ft = (xcd & 1) * 7 + (idx % 7);
  const int rb = (xcd >> 1) * 4 + (idx / 7);

  STAMPW(3, 0);
  const char* asrc = (const char*)hln_f + (size_t)rb * 131072 + tid * 16;
  u32x4 ast[2][4];
#pragma unroll
  for (int i = 0; i < 4; ++i) ast[0][i] = *(const u32x4*)(asrc + i * 8192);
  const char* wbase = (const char*)Wgu_f + (size_t)((ft * 8 + wave) * 2) * 16 * 1024 + lane * 16;
  constexpr int PF = 5;
  bf16x8 wf[PF + 1][2];
#pragma unroll
  for (int p = 0; p < PF; ++p)
#pragma unroll
    for (int j = 0; j < 2; ++j) wf[p][j] = *(const bf16x8*)(wbase + (j * 16 + p) * 1024);
#pragma unroll
  for (int i = 0; i < 4; ++i) ast[1][i] = *(const u32x4*)(asrc + 32768 + i * 8192);

  f32x4 acc[2][8];
#pragma unroll
  for (int j = 0; j < 2; ++j)
#pragma unroll
    for (int m = 0; m < 8; ++m) acc[j][m] = f32x4{0.f, 0.f, 0.f, 0.f};

  bf16x8 xf[2][8];
#pragma unroll
  for (int ch = 0; ch < 4; ++ch) {
    __builtin_amdgcn_sched_barrier(0);
#pragma unroll
    for (int i = 0; i < 4; ++i) *(u32x4*)(smem + ch * 32768 + i * 8192 + tid * 16) = ast[ch & 1][i];
    if (ch + 2 < 4) {
#pragma unroll
      for (int i = 0; i < 4; ++i) ast[ch & 1][i] = *(const u32x4*)(asrc + (ch + 2) * 32768 + i * 8192);
    }
    __syncthreads();
    if (ch == 0) STAMPW(3, 1);
#pragma unroll
    for (int m = 0; m < 8; ++m) xf[0][m] = *(const bf16x8*)(smem + (ch * 32 + m) * 1024 + lane * 16);
    __builtin_amdgcn_sched_barrier(0);
#pragma unroll
    for (int k4 = 0; k4 < 4; ++k4) {
      const int kb = ch * 4 + k4;
      if (kb + PF < 16) {
#pragma unroll
        for (int j = 0; j < 2; ++j)
          wf[(kb + PF) % (PF + 1)][j] = *(const bf16x8*)(wbase + (j * 16 + kb + PF) * 1024);
      }
      if (k4 + 1 < 4) {
#pragma unroll
        for (int m = 0; m < 8; ++m)
          xf[(k4 + 1) & 1][m] = *(const bf16x8*)(smem + ((kb + 1) * 8 + m) * 1024 + lane * 16);
      }
#pragma unroll
      for (int m = 0; m < 8; ++m)
#pragma unroll
        for (int j = 0; j < 2; ++j)
          acc[j][m] = __builtin_amdgcn_mfma_f32_16x16x32_bf16(wf[kb % (PF + 1)][j], xf[k4 & 1][m], acc[j][m], 0, 0, 0);
#pragma unroll
      for (int g = 0; g < 2; ++g) {
        SGB(0x8, 2); SGB(0x100, 1); SGB(0x8, 2); SGB(0x100, 1); SGB(0x8, 2); SGB(0x100, 1); SGB(0x8, 2); SGB(0x100, 1); SGB(0x20, 1);
      }
      __builtin_amdgcn_sched_barrier(0);
    }
  }
  STAMPW(3, 2);
  const int qd = lane >> 4, sl = lane & 15;
  char* obase = (char*)hh_f + (size_t)(ft * 4 + (wave >> 1)) * 1024 + (wave & 1) * 512 + ((qd >> 1) * 16 + sl) * 16 +
                (qd & 1) * 8;
#pragma unroll
  for (int m = 0; m < 8; ++m) {
    float o[4];
#pragma unroll
    for (int r = 0; r < 4; ++r) {
      float gt = rbf(acc[0][m][r]);
      float up = rbf(acc[1][m][r]);
      gt = gt * (gt > 0.f ? 1.f : 0.f);
      o[r] = gt * up;
    }
    u32x2 pk = {pack2(o[0], o[1]), pack2(o[2], o[3])};
    *(u32x2*)(obase + (size_t)(rb * 8 + m) * 56 * 1024) = pk;
  }
  STAMPW(3, 3);
}

__global__ __launch_bounds__(256) void k3_gemm2(const u16* __restrict__ hh_f, const u16* __restrict__ Wd_f,
                                                const u16* __restrict__ hin, u16* __restrict__ out) {
  __shared__ __attribute__((aligned(16))) char red[4 * 16 * 1024];
  const int tid = threadIdx.x;
  const int wave = tid >> 6, lane = tid & 63;
  const int bid = blockIdx.x;
  const int xcd = bid & 7, idx = bid >> 3;
  const int dt = idx & 7, st = xcd * 4 + (idx >> 3);

  STAMPW(4, 0);
  const char* wb = (const char*)Wd_f + (size_t)(dt * 4 * 56 + wave * 14) * 1024 + lane * 16;
  const char* xb = (const char*)hh_f + (size_t)(st * 4 * 56 + wave * 14) * 1024 + lane * 16;
  const int rot = 0;
  auto koff = [&](int kb) { int x = kb + rot; return (x >= 14 ? x - 14 : x) * 1024; };
  constexpr int PF = 5;
  bf16x8 wf[PF + 1][4], xf[PF + 1][4];
#pragma unroll
  for (int p = 0; p < PF; ++p)
#pragma unroll
    for (int j = 0; j < 4; ++j) {
      wf[p][j] = *(const bf16x8*)(wb + j * 56 * 1024 + koff(p));
      xf[p][j] = *(const bf16x8*)(xb + j * 56 * 1024 + koff(p));
    }
  const int qd = lane >> 4, sl = lane & 15;
  u32x2 hv[4];
#pragma unroll
  for (int j = 0; j < 4; ++j)
    hv[j] = *(const u32x2*)(hin + (size_t)(st * 64 + j * 16 + sl) * D_ + dt * 64 + wave * 16 + qd * 4);
  f32x4 acc[4][4];
#pragma unroll
  for (int i = 0; i < 4; ++i)
#pragma unroll
    for (int j = 0; j < 4; ++j) acc[i][j] = f32x4{0.f, 0.f, 0.f, 0.f};
  __builtin_amdgcn_sched_barrier(0);
#pragma unroll
  for (int kb = 0; kb < 14; ++kb) {
    if (kb + PF < 14) {
#pragma unroll
      for (int j = 0; j < 4; ++j) {
        wf[(kb + PF) % (PF + 1)][j] = *(const bf16x8*)(wb + j * 56 * 1024 + koff(kb + PF));
        xf[(kb + PF) % (PF + 1)][j] = *(const bf16x8*)(xb + j * 56 * 1024 + koff(kb + PF));
      }
    }
#pragma unroll
    for (int i = 0; i < 4; ++i)
#pragma unroll
      for (int j = 0; j < 4; ++j)
        acc[i][j] = __builtin_amdgcn_mfma_f32_16x16x32_bf16(wf[kb % (PF + 1)][i], xf[kb % (PF + 1)][j], acc[i][j], 0, 0, 0);
#pragma unroll
    for (int g = 0; g < 8; ++g) { SGB(0x8, 2); SGB(0x20, 1); }
    __builtin_amdgcn_sched_barrier(0);
  }
  STAMPW(4, 1);
#pragma unroll
  for (int i = 0; i < 4; ++i)
#pragma unroll
    for (int j = 0; j < 4; ++j)
      *(f32x4*)(red + ((wave * 4 + i) * 4 + j) * 1024 + lane * 16) = acc[i][j];
  __syncthreads();
#pragma unroll
  for (int j = 0; j < 4; ++j) {
    f32x4 sum = f32x4{0.f, 0.f, 0.f, 0.f};
#pragma unroll
    for (int w = 0; w < 4; ++w) sum += *(const f32x4*)(red + ((w * 4 + wave) * 4 + j) * 1024 + lane * 16);
    float hx[4] = {bflo(hv[j][0]), bfhi(hv[j][0]), bflo(hv[j][1]), bfhi(hv[j][1])};
    float o[4];
#pragma unroll
    for (int r = 0; r < 4; ++r) o[r] = hx[r] + rbf(sum[r]);
    u32x2 pk = {pack2(o[0], o[1]), pack2(o[2], o[3])};
    *(u32x2*)(out + (size_t)(st * 64 + j * 16 + sl) * D_ + dt * 64 + wave * 16 + qd * 4) = pk;
  }
  STAMPW(4, 2);
}

extern "C" void kernel_launch(void* const* d_in, const int* in_sizes, int n_in, void* d_out, int out_size,
                              void* d_ws, size_t ws_size, hipStream_t stream) {
  const u16* q = (const u16*)d_in[0];
  const u16* k = (const u16*)d_in[1];
  const u16* v = (const u16*)d_in[2];
  const u16* shiftk = (const u16*)d_in[3];
  const u16* ssmk = (const u16*)d_in[4];
  const u16* ssmD = (const u16*)d_in[5];
  const u16* Wg = (const u16*)d_in[7];
  const u16* Wu = (const u16*)d_in[8];
  const u16* Wd = (const u16*)d_in[9];
  char* ws = (char*)d_ws;
  u16* y2T = (u16*)(ws);
  u16* hbuf = (u16*)(ws + (2u << 20));
  u16* hln_f = (u16*)(ws + (4u << 20));
  u16* Wgu_f = (u16*)(ws + (6u << 20));
  u16* Wd_f = (u16*)(ws + (10u << 20));
  u16* hh_f = (u16*)(ws + (12u << 20));
  u16* kvT = (u16*)(ws + (20u << 20));

  (void)hipFuncSetAttribute((const void*)k1_conv, hipFuncAttributeMaxDynamicSharedMemorySize, K1_LDS);
  (void)hipFuncSetAttribute((const void*)k2_gemm1, hipFuncAttributeMaxDynamicSharedMemorySize, 131072);

  k0_pre<<<256, 256, 0, stream>>>(k, v, shiftk, kvT);
  k1_conv<<<256, 448, K1_LDS, stream>>>(kvT, ssmk, ssmD, y2T, Wg, Wu, Wd, Wgu_f, Wd_f);
  k1b_rows<<<256, 512, 0, stream>>>(q, y2T, hbuf, hln_f);
  k2_gemm1<<<224, 512, 131072, stream>>>(hln_f, Wgu_f, hh_f);
  k3_gemm2<<<256, 256, 0, stream>>>(hh_f, Wd_f, hbuf, (u16*)d_out);
}
```

```cpp
#include <hip/hip_runtime.h>
#include <hip/hip_bf16.h>

typedef unsigned short u16;
typedef unsigned int u32;
using bf16x8 = __attribute__((ext_vector_type(8))) __bf16;
using f32x4 = __attribute__((ext_vector_type(4))) float;
using f32x16 = __attribute__((ext_vector_type(16))) float;
using u32x4 = __attribute__((ext_vector_type(4))) u32;
using u32x2 = __attribute__((ext_vector_type(2))) u32;
using s16x4 = __attribute__((ext_vector_type(4))) short;
#define LDS_PTR __attribute__((address_space(3)))

constexpr int S_ = 2048, D_ = 512, F_ = 1792;

#define STAMPW(kid, idx)

__device__ __forceinline__ float bf2f(u32 u) { return __uint_as_float(u << 16); }
__device__ __forceinline__ float bflo(u32 u) { return __uint_as_float(u << 16); }
__device__ __forceinline__ float bfhi(u32 u) { return __uint_as_float(u & 0xffff0000u); }
__device__ __forceinline__ u32 f2bf(float f) {
  __bf16 b = (__bf16)f;
  return (u32)__builtin_bit_cast(u16, b);
}
__device__ __forceinline__ float rbf(float f) { return bf2f(f2bf(f)); }
__device__ __forceinline__ u32 pack2(float lo, float hi) { return f2bf(lo) | (f2bf(hi) << 16); }

__global__ __launch_bounds__(256) void k0_pre(const u16* __restrict__ kin, const u16* __restrict__ vin,
                                              const u16* __restrict__ shiftk, u16* __restrict__ kvT,
                                              u32* __restrict__ cnt) {
  const int tid = threadIdx.x;
  const int bid = blockIdx.x;
  if (bid == 0) {
    for (int i = tid; i < 32 * 64; i += 256) cnt[i] = 0u;
  }
  STAMPW(0, 0);
  const int ct = bid & 7, rt = bid >> 3;
  const int d0 = ct * 64 + (tid & 31) * 2;
  const int r0 = rt * 64 + (tid >> 5) * 8;
  u32 kw[11], vw[8];
  {
    const u32* kp = (const u32*)(kin + d0);
    const u32* vp = (const u32*)(vin + d0);
#pragma unroll
    for (int i = 0; i < 11; ++i) {
      int r = r0 - 3 + i;
      kw[i] = (r >= 0) ? kp[(size_t)r * (D_ / 2)] : 0u;
    }
#pragma unroll
    for (int i = 0; i < 8; ++i) vw[i] = vp[(size_t)(r0 + i) * (D_ / 2)];
  }
  const u32x2 sw0 = *(const u32x2*)(shiftk + (size_t)d0 * 4);
  const u32x2 sw1 = *(const u32x2*)(shiftk + (size_t)(d0 + 1) * 4);
  {
    float w[2][4] = {{bflo(sw0[0]), bfhi(sw0[0]), bflo(sw0[1]), bfhi(sw0[1])},
                     {bflo(sw1[0]), bfhi(sw1[0]), bflo(sw1[1]), bfhi(sw1[1])}};
#pragma unroll
    for (int c = 0; c < 2; ++c) {
      float kf[11];
#pragma unroll
      for (int i = 0; i < 11; ++i) kf[i] = c ? bfhi(kw[i]) : bflo(kw[i]);
      float u[8];
#pragma unroll
      for (int i = 0; i < 8; ++i) {
        float acc = rbf(kf[i] * w[c][3]);
        acc = rbf(acc + rbf(kf[i + 1] * w[c][2]));
        acc = rbf(acc + rbf(kf[i + 2] * w[c][1]));
        acc = rbf(acc + rbf(kf[i + 3] * w[c][0]));
        float vf = c ? bfhi(vw[i]) : bflo(vw[i]);
        u[i] = acc * vf;
      }
      u32x4 pk = {pack2(u[0], u[1]), pack2(u[2], u[3]), pack2(u[4], u[5]), pack2(u[6], u[7])};
      *(u32x4*)(kvT + (size_t)(d0 + c) * S_ + r0) = pk;
    }
  }
  STAMPW(0, 1);
}

constexpr int KCS = 4160;
constexpr int KCH = 8 * KCS;
constexpr int UBS = 80;
constexpr int UCH = 96 * UBS;
constexpr int K1_KOFF = 0;
constexpr int K1_UOFF = 2 * KCH;
constexpr int K1_ROFF = K1_UOFF + 2 * UCH;
constexpr int K1_WOFF = K1_ROFF + 2 * 4096;
constexpr int K1_LDS = K1_WOFF + 3 * 32 * 272;

__device__ __forceinline__ u32 rot16(u32 x) { return (x >> 16) | (x << 16); }

__device__ __forceinline__ constexpr int k1_boff(int j) {
  return ((((16 - 16 * j) + 2048) >> 5) - 64 + 32) * UBS + (((16 - 16 * j) + 2048) & 31) * 2;
}

__global__ __launch_bounds__(448) void k1_conv(const u16* __restrict__ kvT, const u16* __restrict__ ssmk,
                                               const u16* __restrict__ ssmD, u16* __restrict__ y2T,
                                               const u16* __restrict__ Wg, const u16* __restrict__ Wu,
                                               const u16* __restrict__ Wd, u16* __restrict__ Wgu_f,
                                               u16* __restrict__ Wd_f) {
  extern __shared__ __attribute__((aligned(16))) char smem[];
  const int tid = threadIdx.x;
  const int d0 = 2 * blockIdx.x;
  const int bid = blockIdx.x;
  const int wave = __builtin_amdgcn_readfirstlane(tid >> 6), lane = tid & 63;
  STAMPW(1, 0);
  if (wave >= 4) {
    const int wtask = bid * 3 + (wave - 4);
    __builtin_amdgcn_s_barrier();
    u32x4 rws[8];
    char* wdst = nullptr;
    int wstride = 0;
    if (wtask < 672) {
      const int which = wtask / 224, rem = wtask - which * 224;
      const u16* src;
      int ld, kb, nt;
      if (which < 2) {
        kb = rem / 14; nt = rem - kb * 14; ld = F_;
        src = (which == 0 ? Wg : Wu);
        wdst = (char*)Wgu_f + (size_t)((nt * 16 + which) * 16 + kb) * 1024;
        wstride = 32 * 1024;
      } else {
        kb = rem >> 2; nt = rem & 3; ld = D_;
        src = Wd;
        wdst = (char*)Wd_f + (size_t)(nt * 8 * 56 + kb) * 1024;
        wstride = 56 * 1024;
      }
      const u16* sp = src + (size_t)(kb * 32 + (lane >> 4)) * ld + nt * 128 + (lane & 15) * 8;
#pragma unroll
      for (int i = 0; i < 8; ++i) rws[i] = *(const u32x4*)(sp + (size_t)(4 * i) * ld);
    }
    if (wtask < 672) {
      char* wl = smem + K1_WOFF + (wave - 4) * (32 * 272);
#pragma unroll
      for (int i = 0; i < 8; ++i) *(u32x4*)(wl + (4 * i + (lane >> 4)) * 272 + (lane & 15) * 16) = rws[i];
      const char* rp = wl + (8 * (lane >> 4) + ((lane & 15) >> 2)) * 272 + (lane & 3) * 8;
#pragma unroll
      for (int j = 0; j < 8; ++j) {
        s16x4 lo = __builtin_amdgcn_ds_read_tr16_b64_v4i16((LDS_PTR s16x4*)(rp + j * 32));
        s16x4 hi = __builtin_amdgcn_ds_read_tr16_b64_v4i16((LDS_PTR s16x4*)(rp + j * 32 + 4 * 272));
        u32x2 l2 = __builtin_bit_cast(u32x2, lo), h2 = __builtin_bit_cast(u32x2, hi);
        *(u32x4*)(wdst + (size_t)j * wstride + lane * 16) = u32x4{l2[0], l2[1], h2[0], h2[1]};
      }
    }
    __builtin_amdgcn_s_barrier();
    return;
  }
  u32x4 g1[2], g0[2], uv[2];
#pragma unroll
  for (int c = 0; c < 2; ++c) {
    const u16* kr = ssmk + (size_t)(d0 + c) * S_;
    int a = 2040 - 8 * tid;
    g1[c] = *(const u32x4*)(kr + a);
    if (a >= 8) g0[c] = *(const u32x4*)(kr + a - 8);
    else g0[c] = u32x4{0, 0, 0, 0};
    uv[c] = *(const u32x4*)(kvT + (size_t)(d0 + c) * S_ + tid * 8);
  }
  const float dval0 = bf2f(ssmD[d0]), dval1 = bf2f(ssmD[d0 + 1]);
  {
    u32x4 z = {0, 0, 0, 0};
    for (int i = tid; i < 2 * 160; i += 256) {
      int c = i / 160, j = i - c * 160;
      *(u32x4*)(smem + K1_UOFF + c * UCH + j * 16) = z;
    }
    if (tid < 64) {
      int c = tid >> 5, r = (tid >> 2) & 7, q = 256 + (tid & 3);
      *(u32x4*)(smem + K1_KOFF + c * KCH + r * KCS + q * 16) = z;
    }
  }
  STAMPW(1, 1);
#pragma unroll
  for (int c = 0; c < 2; ++c) {
    u32 W[8], O[7];
#pragma unroll
    for (int i = 0; i < 4; ++i) {
      W[i] = rot16(g1[c][3 - i]);
      W[4 + i] = rot16(g0[c][3 - i]);
    }
#pragma unroll
    for (int i = 0; i < 7; ++i) O[i] = (W[i] >> 16) | (W[i + 1] << 16);
    char* base = smem + K1_KOFF + c * KCH + tid * 16;
#pragma unroll
    for (int r = 0; r < 8; ++r) {
      u32x4 val;
      if ((r & 1) == 0) val = u32x4{W[r / 2], W[r / 2 + 1], W[r / 2 + 2], W[r / 2 + 3]};
      else val = u32x4{O[r / 2], O[r / 2 + 1], O[r / 2 + 2], O[r / 2 + 3]};
      *(u32x4*)(base + r * KCS) = val;
    }
    *(u32x4*)(smem + K1_UOFF + c * UCH + (32 + (tid >> 2)) * UBS + (tid & 3) * 16) = uv[c];
  }
  __syncthreads();
  STAMPW(1, 2);

  const int c = wave >> 1, hf = wave & 1;
  const int b = lane & 31, h = lane >> 5;
  f32x16 acc0, acc1;
#pragma unroll
  for (int i = 0; i < 16; ++i) { acc0[i] = 0.f; acc1[i] = 0.f; }
  const int i0 = 2047 + 16 - b + 8 * h;
  const int r = i0 & 7;
  const char* abase = smem + K1_KOFF + c * KCH + r * KCS + (i0 - r) * 2;
  const char* ulo = smem + K1_UOFF + c * UCH + b * UBS + 16 * h;
  const char* uhi = ulo + 32 * UBS;
  constexpr int PD = 3;
  if (hf == 0) {
    const char* ap = abase - 32 * 63;
    bf16x8 af[PD + 1], bl[PD + 1], bh[PD + 1];
#pragma unroll
    for (int j = 0; j < PD; ++j) {
      af[j] = *(const bf16x8*)(ap + 32 * (63 - j));
      bl[j] = *(const bf16x8*)(ulo + k1_boff(j));
      bh[j] = *(const bf16x8*)(uhi + k1_boff(j));
    }
#pragma unroll
    for (int j = 0; j < 64; ++j) {
      if (j + PD < 64) {
        af[(j + PD) % (PD + 1)] = *(const bf16x8*)(ap + 32 * (63 - j - PD));
        bl[(j + PD) % (PD + 1)] = *(const bf16x8*)(ulo + k1_boff(j + PD));
        if (j + PD < 32) bh[(j + PD) % (PD + 1)] = *(const bf16x8*)(uhi + k1_boff(j + PD));
      }
      acc0 = __builtin_amdgcn_mfma_f32_32x32x16_bf16(af[j % (PD + 1)], bl[j % (PD + 1)], acc0, 0, 0, 0);
      if (j < 32) acc1 = __builtin_amdgcn_mfma_f32_32x32x16_bf16(af[j % (PD + 1)], bh[j % (PD + 1)], acc1, 0, 0, 0);
      __builtin_amdgcn_sched_group_barrier(0x8, 1, 0);
      __builtin_amdgcn_sched_group_barrier(0x100, 2, 0);
      __builtin_amdgcn_sched_group_barrier(0x8, 1, 0);
      __builtin_amdgcn_sched_group_barrier(0x100, 1, 0);
      __builtin_amdgcn_sched_barrier(0);
    }
  } else {
    const char* ap = abase - 32 * 127;
    bf16x8 af[PD + 1], bh[PD + 1];
#pragma unroll
    for (int j = 0; j < PD; ++j) {
      af[j] = *(const bf16x8*)(ap + 32 * (127 - 32 - j));
      bh[j] = *(const bf16x8*)(uhi + k1_boff(32 + j));
    }
#pragma unroll
    for (int j = 0; j < 96; ++j) {
      if (j + PD < 96) {
        af[(j + PD) % (PD + 1)] = *(const bf16x8*)(ap + 32 * (127 - 32 - j - PD));
        bh[(j + PD) % (PD + 1)] = *(const bf16x8*)(uhi + k1_boff(32 + j + PD));
      }
      acc0 = __builtin_amdgcn_mfma_f32_32x32x16_bf16(af[j % (PD + 1)], bh[j % (PD + 1)], acc0, 0, 0, 0);
      __builtin_amdgcn_sched_group_barrier(0x8, 1, 0);
      __builtin_amdgcn_sched_group_barrier(0x100, 2, 0);
      __builtin_amdgcn_sched_barrier(0);
    }
  }
  STAMPW(1, 3);
  {
    float* xch = (float*)(smem + K1_ROFF + c * 4096);
    if (hf == 0) {
#pragma unroll
      for (int i = 0; i < 4; ++i) {
        f32x4 x;
#pragma unroll
        for (int j = 0; j < 4; ++j) x[j] = acc1[4 * i + j];
        *(f32x4*)(xch + (i * 64 + lane) * 4) = x;
      }
    }
    __syncthreads();
    if (hf == 1) {
#pragma unroll
      for (int i = 0; i < 4; ++i) {
        f32x4 x = *(const f32x4*)(xch + (i * 64 + lane) * 4);
#pragma unroll
        for (int j = 0; j < 4; ++j) acc0[4 * i + j] += x[j];
      }
    }
    const float dval = c ? dval1 : dval0;
    const int A = b + 32 * hf;
    u16* orow = y2T + (size_t)(d0 + c) * 8 + 4 * h;
#pragma unroll
    for (int i = 0; i < 4; ++i) {
      u32x2 kvp = *(const u32x2*)(smem + K1_UOFF + c * UCH + (32 + A) * UBS + (8 * i + 4 * h) * 2);
      float kv[4] = {bflo(kvp[0]), bfhi(kvp[0]), bflo(kvp[1]), bfhi(kvp[1])};
      float o[4];
#pragma unroll
      for (int j = 0; j < 4; ++j) o[j] = rbf(acc0[4 * i + j]) + rbf(kv[j] * dval);
      u32x2 pk = {pack2(o[0], o[1]), pack2(o[2], o[3])};
      *(u32x2*)(orow + (size_t)(4 * A + i) * (D_ * 8)) = pk;
    }
  }
  STAMPW(1, 4);
}

template <int CTRL>
__device__ __forceinline__ float dpp_add(float x) {
  int y = __builtin_amdgcn_update_dpp(0, __float_as_int(x), CTRL, 0xf, 0xf, true);
  return x + __int_as_float(y);
}
__device__ __forceinline__ float wave_sum(float x) {
  x = dpp_add<0xB1>(x);
  x = dpp_add<0x4E>(x);
  x = dpp_add<0x141>(x);
  x = dpp_add<0x140>(x);
  int xi = __float_as_int(x);
  float a = __int_as_float(__builtin_amdgcn_readlane(xi, 0));
  float b = __int_as_float(__builtin_amdgcn_readlane(xi, 16));
  float c = __int_as_float(__builtin_amdgcn_readlane(xi, 32));
  float d = __int_as_float(__builtin_amdgcn_readlane(xi, 48));
  return (a + b) + (c + d);
}

__device__ __forceinline__ void ln_bf16(const float (&x)[8], float (&o)[8]) {
  float s = ((x[0] + x[1]) + (x[2] + x[3])) + ((x[4] + x[5]) + (x[6] + x[7]));
  const float mean = wave_sum(s) * (1.f / 512.f);
  float ss = 0.f;
#pragma unroll
  for (int i = 0; i < 8; ++i) { float dlt = x[i] - mean; ss += dlt * dlt; }
  const float var = wave_sum(ss) * (1.f / 512.f);
  const float mb = rbf(mean);
  const float vb = rbf(var);
  const float sd = rbf(sqrtf(rbf(vb + 1e-5f)));
  const float rc = 1.0f / sd;
#pragma unroll
  for (int i = 0; i < 8; ++i) {
    float a = rbf(x[i] - mb);
    float qt = a * rc;
    float rem = __builtin_fmaf(-qt, sd, a);
    qt = __builtin_fmaf(rem, rc, qt);
    o[i] = rbf(qt);
  }
}

#define SGB(mask, n) __builtin_amdgcn_sched_group_barrier(mask, n, 0)
#define AUX_SC1 16

__device__ __forceinline__ void wait_count(const u32* cnt, u32 target) {
  if (threadIdx.x == 0) {
    int spins = 0;
    while (__hip_atomic_load(cnt, __ATOMIC_RELAXED, __HIP_MEMORY_SCOPE_AGENT) < target) {
      __builtin_amdgcn_s_sleep(1);
      if (++spins > (1 << 22)) break;
    }
  }
  __syncthreads();
}
__device__ __forceinline__ void signal_count(u32* cnt) {
  asm volatile("s_waitcnt vmcnt(0)" ::: "memory");
  __syncthreads();
  if (threadIdx.x == 0) __hip_atomic_fetch_add(cnt, 1u, __ATOMIC_RELAXED, __HIP_MEMORY_SCOPE_AGENT);
}

__global__ __launch_bounds__(512) void k234(const u16* __restrict__ q, const u16* __restrict__ y2T,
                                            u16* hbuf, u16* hln_f, const u16* __restrict__ Wgu_f,
                                            u16* hh_f, const u16* __restrict__ Wd_f, u16* __restrict__ out,
                                            u32* cnt) {
  extern __shared__ __attribute__((aligned(16))) char smem[];
  const int tid = threadIdx.x;
  const int wave = tid >> 6, lane = tid & 63;
  const int bid = blockIdx.x;
  const int xcd = bid & 7, idx = bid >> 3;
  const auto rs_h = __builtin_amdgcn_make_buffer_rsrc((void*)hbuf, 0, S_ * D_ * 2, 0x00020000);
  const auto rs_hln = __builtin_amdgcn_make_buffer_rsrc((void*)hln_f, 0, S_ * D_ * 2, 0x00020000);
  const auto rs_hh = __builtin_amdgcn_make_buffer_rsrc((void*)hh_f, 0, S_ * F_ * 2, 0x00020000);
  u32* cnt1 = cnt;
  u32* cnt2 = cnt + 16 * 64;

  STAMPW(2, 0);
  {
    u16(*tile)[528] = (u16(*)[528])smem;
    const int s0 = bid * 8;
    u32x4 ya = *(const u32x4*)(y2T + (size_t)bid * (D_ * 8) + tid * 8);
    u32x4 qv = *(const u32x4*)(q + (size_t)(s0 + wave) * D_ + lane * 8);
#pragma unroll
    for (int i = 0; i < 4; ++i) {
      tile[2 * i][tid] = (u16)(ya[i] & 0xffffu);
      tile[2 * i + 1][tid] = (u16)(ya[i] >> 16);
    }
    float x[8], ql[8];
#pragma unroll
    for (int i = 0; i < 4; ++i) {
      x[2 * i] = bflo(qv[i]);
      x[2 * i + 1] = bfhi(qv[i]);
    }
    ln_bf16(x, ql);
    __syncthreads();
    float hh[8], hl[8];
    {
      u32x4 yv = *(const u32x4*)(&tile[wave][lane * 8]);
#pragma unroll
      for (int i = 0; i < 4; ++i) {
        hh[2 * i] = rbf(x[2 * i] + rbf(ql[2 * i] * bflo(yv[i])));
        hh[2 * i + 1] = rbf(x[2 * i + 1] + rbf(ql[2 * i + 1] * bfhi(yv[i])));
      }
    }
    const int s = s0 + wave;
    u32x4 hp = {pack2(hh[0], hh[1]), pack2(hh[2], hh[3]), pack2(hh[4], hh[5]), pack2(hh[6], hh[7])};
    __builtin_amdgcn_raw_buffer_store_b128(hp, rs_h, (s * D_ + lane * 8) * 2, 0, AUX_SC1);
    ln_bf16(hh, hl);
    u32x4 lp = {pack2(hl[0], hl[1]), pack2(hl[2], hl[3]), pack2(hl[4], hl[5]), pack2(hl[6], hl[7])};
    const int off = (((s >> 7) * 16 + (lane >> 2)) * 8 + ((s >> 4) & 7)) * 1024 + ((lane & 3) * 16 + (s & 15)) * 16;
    __builtin_amdgcn_raw_buffer_store_b128(lp, rs_hln, off, 0, AUX_SC1);
    signal_count(cnt1 + (bid >> 4) * 64);
  }
  STAMPW(2, 1);

  if (bid < 224) {
    const int ft = (xcd & 1) * 7 + (idx % 7);
    const int rb = (xcd >> 1) * 4 + (idx / 7);
    const char* wbase = (const char*)Wgu_f + (size_t)((ft * 8 + wave) * 2) * 16 * 1024 + lane * 16;
    constexpr int PF = 5;
    bf16x8 wf[PF + 1][2];
#pragma unroll
    for (int p = 0; p < PF; ++p)
#pragma unroll
      for (int j = 0; j < 2; ++j) wf[p][j] = *(const bf16x8*)(wbase + (j * 16 + p) * 1024);
    wait_count(cnt1 + rb * 64, 16);
    STAMPW(2, 2);
    const int aoff = rb * 131072 + tid * 16;
    u32x4 ast[2][4];
#pragma unroll
    for (int i = 0; i < 4; ++i) ast[0][i] = __builtin_amdgcn_raw_buffer_load_b128(rs_hln, aoff + i * 8192, 0, AUX_SC1);
#pragma unroll
    for (int i = 0; i < 4; ++i)
      ast[1][i] = __builtin_amdgcn_raw_buffer_load_b128(rs_hln, aoff + 32768 + i * 8192, 0, AUX_SC1);

    f32x4 acc[2][8];
#pragma unroll
    for (int j = 0; j < 2; ++j)
#pragma unroll
      for (int m = 0; m < 8; ++m) acc[j][m] = f32x4{0.f, 0.f, 0.f, 0.f};

    bf16x8 xf[2][8];
#pragma unroll
    for (int ch = 0; ch < 4; ++ch) {
      __builtin_amdgcn_sched_barrier(0);
#pragma unroll
      for (int i = 0; i < 4; ++i) *(u32x4*)(smem + ch * 32768 + i * 8192 + tid * 16) = ast[ch & 1][i];
      if (ch + 2 < 4) {
#pragma unroll
        for (int i = 0; i < 4; ++i)
          ast[ch & 1][i] = __builtin_amdgcn_raw_buffer_load_b128(rs_hln, aoff + (ch + 2) * 32768 + i * 8192, 0, AUX_SC1);
      }
      __syncthreads();
#pragma unroll
      for (int m = 0; m < 8; ++m) xf[0][m] = *(const bf16x8*)(smem + (ch * 32 + m) * 1024 + lane * 16);
      __builtin_amdgcn_sched_barrier(0);
#pragma unroll
      for (int k4 = 0; k4 < 4; ++k4) {
        const int kb = ch * 4 + k4;
        if (kb + PF < 16) {
#pragma unroll
          for (int j = 0; j < 2; ++j)
            wf[(kb + PF) % (PF + 1)][j] = *(const bf16x8*)(wbase + (j * 16 + kb + PF) * 1024);
        }
        if (k4 + 1 < 4) {
#pragma unroll
          for (int m = 0; m < 8; ++m)
            xf[(k4 + 1) & 1][m] = *(const bf16x8*)(smem + ((kb + 1) * 8 + m) * 1024 + lane * 16);
        }
#pragma unroll
        for (int m = 0; m < 8; ++m)
#pragma unroll
          for (int j = 0; j < 2; ++j)
            acc[j][m] = __builtin_amdgcn_mfma_f32_16x16x32_bf16(wf[kb % (PF + 1)][j], xf[k4 & 1][m], acc[j][m], 0, 0, 0);
#pragma unroll
        for (int g = 0; g < 2; ++g) {
          SGB(0x8, 2); SGB(0x100, 1); SGB(0x8, 2); SGB(0x100, 1); SGB(0x8, 2); SGB(0x100, 1); SGB(0x8, 2); SGB(0x100, 1); SGB(0x20, 1);
        }
        __builtin_amdgcn_sched_barrier(0);
      }
    }
    STAMPW(2, 3);
    __syncthreads();
    {
      const int qd = lane >> 4, sl = lane & 15;
      char* ob = smem + (wave >> 1) * 1024 + (wave & 1) * 512 + ((qd >> 1) * 16 + sl) * 16 + (qd & 1) * 8;
#pragma unroll
      for (int m = 0; m < 8; ++m) {
        float o[4];
#pragma unroll
        for (int r = 0; r < 4; ++r) {
          float gt = rbf(acc[0][m][r]);
          float up = rbf(acc[1][m][r]);
          gt = gt * (gt > 0.f ? 1.f : 0.f);
          o[r] = gt * up;
        }
        u32x2 pk = {pack2(o[0], o[1]), pack2(o[2], o[3])};
        *(u32x2*)(ob + m * 4096) = pk;
      }
    }
    __syncthreads();
#pragma unroll
    for (int i = 0; i < 4; ++i) {
      const int lo = i * 8192 + tid * 16;
      const int m = lo >> 12, rest = lo & 4095;
      u32x4 v = *(const u32x4*)(smem + lo);
      __builtin_amdgcn_raw_buffer_store_b128(v, rs_hh, ((rb * 8 + m) * 56 + ft * 4) * 1024 + rest, 0, AUX_SC1);
    }
    signal_count(cnt2 + rb * 64);
  }
  STAMPW(2, 4);

  {
    const int dt = idx & 7, st = xcd * 4 + (idx >> 3);
    const char* wb = (const char*)Wd_f + (size_t)(dt * 4 * 56 + wave * 7) * 1024 + lane * 16;
    const int xo = (st * 4 * 56 + wave * 7) * 1024 + lane * 16;
    constexpr int PF = 3;
    bf16x8 wf[PF + 1][4], xf[PF + 1][4];
#pragma unroll
    for (int p = 0; p < PF; ++p)
#pragma unroll
      for (int j = 0; j < 4; ++j) wf[p][j] = *(const bf16x8*)(wb + (j * 56 + p) * 1024);
    wait_count(cnt2 + (st >> 1) * 64, 14);
    STAMPW(2, 5);
#pragma unroll
    for (int p = 0; p < PF; ++p)
#pragma unroll
      for (int j = 0; j < 4; ++j)
        xf[p][j] = __builtin_bit_cast(bf16x8, __builtin_amdgcn_raw_buffer_load_b128(rs_hh, xo + (j * 56 + p) * 1024, 0, AUX_SC1));
    const int qd = lane >> 4, sl = lane & 15;
    const int ib = wave >> 1, jb = (wave & 1) * 2;
    u32x2 hv[2];
#pragma unroll
    for (int j = 0; j < 2; ++j)
      hv[j] = __builtin_amdgcn_raw_buffer_load_b64(rs_h, ((st * 64 + (jb + j) * 16 + sl) * D_ + dt * 64 + ib * 16 + qd * 4) * 2, 0, AUX_SC1);
    f32x4 acc[4][4];
#pragma unroll
    for (int i = 0; i < 4; ++i)
#pragma unroll
      for (int j = 0; j < 4; ++j) acc[i][j] = f32x4{0.f, 0.f, 0.f, 0.f};
    __builtin_amdgcn_sched_barrier(0);
#pragma unroll
    for (int kb = 0; kb < 7; ++kb) {
      if (kb + PF < 7) {
#pragma unroll
        for (int j = 0; j < 4; ++j) {
          wf[(kb + PF) % (PF + 1)][j] = *(const bf16x8*)(wb + (j * 56 + kb + PF) * 1024);
          xf[(kb + PF) % (PF + 1)][j] = __builtin_bit_cast(
              bf16x8, __builtin_amdgcn_raw_buffer_load_b128(rs_hh, xo + (j * 56 + kb + PF) * 1024, 0, AUX_SC1));
        }
      }
#pragma unroll
      for (int i = 0; i < 4; ++i)
#pragma unroll
        for (int j = 0; j < 4; ++j)
          acc[i][j] = __builtin_amdgcn_mfma_f32_16x16x32_bf16(wf[kb % (PF + 1)][i], xf[kb % (PF + 1)][j], acc[i][j], 0, 0, 0);
#pragma unroll
      for (int g = 0; g < 8; ++g) { SGB(0x8, 2); SGB(0x20, 1); }
      __builtin_amdgcn_sched_barrier(0);
    }
    STAMPW(2, 6);
#pragma unroll
    for (int i = 0; i < 4; ++i)
#pragma unroll
      for (int j = 0; j < 4; ++j)
        *(f32x4*)(smem + ((wave * 4 + i) * 4 + j) * 1024 + lane * 16) = acc[i][j];
    __syncthreads();
#pragma unroll
    for (int j = 0; j < 2; ++j) {
      f32x4 sum = f32x4{0.f, 0.f, 0.f, 0.f};
#pragma unroll
      for (int w = 0; w < 8; ++w) sum += *(const f32x4*)(smem + ((w * 4 + ib) * 4 + jb + j) * 1024 + lane * 16);
      float hx[4] = {bflo(hv[j][0]), bfhi(hv[j][0]), bflo(hv[j][1]), bfhi(hv[j][1])};
      float o[4];
#pragma unroll
      for (int r = 0; r < 4; ++r) o[r] = hx[r] + rbf(sum[r]);
      u32x2 pk = {pack2(o[0], o[1]), pack2(o[2], o[3])};
      *(u32x2*)(out + (size_t)(st * 64 + (jb + j) * 16 + sl) * D_ + dt * 64 + ib * 16 + qd * 4) = pk;
    }
  }
  STAMPW(2, 7);
}

extern "C" void kernel_launch(void* const* d_in, const int* in_sizes, int n_in, void* d_out, int out_size,
                              void* d_ws, size_t ws_size, hipStream_t stream) {
  const u16* q = (const u16*)d_in[0];
  const u16* k = (const u16*)d_in[1];
  const u16* v = (const u16*)d_in[2];
  const u16* shiftk = (const u16*)d_in[3];
  const u16* ssmk = (const u16*)d_in[4];
  const u16* ssmD = (const u16*)d_in[5];
  const u16* Wg = (const u16*)d_in[7];
  const u16* Wu = (const u16*)d_in[8];
  const u16* Wd = (const u16*)d_in[9];
  char* ws = (char*)d_ws;
  u16* y2T = (u16*)(ws);
  u16* hbuf = (u16*)(ws + (2u << 20));
  u16* hln_f = (u16*)(ws + (4u << 20));
  u16* Wgu_f = (u16*)(ws + (6u << 20));
  u16* Wd_f = (u16*)(ws + (10u << 20));
  u16* hh_f = (u16*)(ws + (12u << 20));
  u16* kvT = (u16*)(ws + (20u << 20));

  u32* cnt = (u32*)(ws + (24u << 20));

  (void)hipFuncSetAttribute((const void*)k1_conv, hipFuncAttributeMaxDynamicSharedMemorySize, K1_LDS);
  (void)hipFuncSetAttribute((const void*)k234, hipFuncAttributeMaxDynamicSharedMemorySize, 131072);

  k0_pre<<<256, 256, 0, stream>>>(k, v, shiftk, kvT, cnt);
  k1_conv<<<256, 448, K1_LDS, stream>>>(kvT, ssmk, ssmD, y2T, Wg, Wu, Wd, Wgu_f, Wd_f);
  k234<<<256, 512, 131072, stream>>>(q, y2T, hbuf, hln_f, Wgu_f, hh_f, Wd_f, (u16*)d_out, cnt);
}
```

```cpp
#include <hip/hip_runtime.h>
#include <hip/hip_bf16.h>

typedef unsigned short u16;
typedef unsigned int u32;
using bf16x8 = __attribute__((ext_vector_type(8))) __bf16;
using f32x4 = __attribute__((ext_vector_type(4))) float;
using f32x16 = __attribute__((ext_vector_type(16))) float;
using u32x4 = __attribute__((ext_vector_type(4))) u32;
using u32x2 = __attribute__((ext_vector_type(2))) u32;
using s16x4 = __attribute__((ext_vector_type(4))) short;
#define LDS_PTR __attribute__((address_space(3)))

constexpr int S_ = 2048, D_ = 512, F_ = 1792;


__device__ __forceinline__ float bf2f(u32 u) { return __uint_as_float(u << 16); }
__device__ __forceinline__ float bflo(u32 u) { return __uint_as_float(u << 16); }
__device__ __forceinline__ float bfhi(u32 u) { return __uint_as_float(u & 0xffff0000u); }
__device__ __forceinline__ u32 f2bf(float f) {
  __bf16 b = (__bf16)f;
  return (u32)__builtin_bit_cast(u16, b);
}
__device__ __forceinline__ float rbf(float f) { return bf2f(f2bf(f)); }
__device__ __forceinline__ u32 pack2(float lo, float hi) { return f2bf(lo) | (f2bf(hi) << 16); }

__global__ __launch_bounds__(256) void k0_pre(const u16* __restrict__ kin, const u16* __restrict__ vin,
                                              const u16* __restrict__ shiftk, u16* __restrict__ kvT,
                                              u32* __restrict__ cnt) {
  const int tid = threadIdx.x;
  const int bid = blockIdx.x;
  if (bid == 0) {
    for (int i = tid; i < 32 * 64; i += 256) cnt[i] = 0u;
  }
  const int ct = bid & 7, rt = bid >> 3;
  const int d0 = ct * 64 + (tid & 31) * 2;
  const int r0 = rt * 64 + (tid >> 5) * 8;
  u32 kw[11], vw[8];
  {
    const u32* kp = (const u32*)(kin + d0);
    const u32* vp = (const u32*)(vin + d0);
#pragma unroll
    for (int i = 0; i < 11; ++i) {
      int r = r0 - 3 + i;
      kw[i] = (r >= 0) ? kp[(size_t)r * (D_ / 2)] : 0u;
    }
#pragma unroll
    for (int i = 0; i < 8; ++i) vw[i] = vp[(size_t)(r0 + i) * (D_ / 2)];
  }
  const u32x2 sw0 = *(const u32x2*)(shiftk + (size_t)d0 * 4);
  const u32x2 sw1 = *(const u32x2*)(shiftk + (size_t)(d0 + 1) * 4);
  {
    float w[2][4] = {{bflo(sw0[0]), bfhi(sw0[0]), bflo(sw0[1]), bfhi(sw0[1])},
                     {bflo(sw1[0]), bfhi(sw1[0]), bflo(sw1[1]), bfhi(sw1[1])}};
#pragma unroll
    for (int c = 0; c < 2; ++c) {
      float kf[11];
#pragma unroll
      for (int i = 0; i < 11; ++i) kf[i] = c ? bfhi(kw[i]) : bflo(kw[i]);
      float u[8];
#pragma unroll
      for (int i = 0; i < 8; ++i) {
        float acc = rbf(kf[i] * w[c][3]);
        acc = rbf(acc + rbf(kf[i + 1] * w[c][2]));
        acc = rbf(acc + rbf(kf[i + 2] * w[c][1]));
        acc = rbf(acc + rbf(kf[i + 3] * w[c][0]));
        float vf = c ? bfhi(vw[i]) : bflo(vw[i]);
        u[i] = acc * vf;
      }
      u32x4 pk = {pack2(u[0], u[1]), pack2(u[2], u[3]), pack2(u[4], u[5]), pack2(u[6], u[7])};
      *(u32x4*)(kvT + (size_t)(d0 + c) * S_ + r0) = pk;
    }
  }
}

constexpr int KCS = 4160;
constexpr int KCH = 8 * KCS;
constexpr int UBS = 80;
constexpr int UCH = 96 * UBS;
constexpr int K1_KOFF = 0;
constexpr int K1_UOFF = 2 * KCH;
constexpr int K1_ROFF = K1_UOFF + 2 * UCH;
constexpr int K1_WOFF = K1_ROFF + 2 * 4096;
constexpr int K1_LDS = K1_WOFF + 3 * 32 * 272;

__device__ __forceinline__ u32 rot16(u32 x) { return (x >> 16) | (x << 16); }

__device__ __forceinline__ constexpr int k1_boff(int j) {
  return ((((16 - 16 * j) + 2048) >> 5) - 64 + 32) * UBS + (((16 - 16 * j) + 2048) & 31) * 2;
}

__global__ __launch_bounds__(448) void k1_conv(const u16* __restrict__ kvT, const u16* __restrict__ ssmk,
                                               const u16* __restrict__ ssmD, u16* __restrict__ y2T,
                                               const u16* __restrict__ Wg, const u16* __restrict__ Wu,
                                               const u16* __restrict__ Wd, u16* __restrict__ Wgu_f,
                                               u16* __restrict__ Wd_f) {
  extern __shared__ __attribute__((aligned(16))) char smem[];
  const int tid = threadIdx.x;
  const int d0 = 2 * blockIdx.x;
  const int bid = blockIdx.x;
  const int wave = __builtin_amdgcn_readfirstlane(tid >> 6), lane = tid & 63;
  if (wave >= 4) {
    const int wtask = bid * 3 + (wave - 4);
    __builtin_amdgcn_s_barrier();
    u32x4 rws[8];
    char* wdst = nullptr;
    int wstride = 0;
    if (wtask < 672) {
      const int which = wtask / 224, rem = wtask - which * 224;
      const u16* src;
      int ld, kb, nt;
      if (which < 2) {
        kb = rem / 14; nt = rem - kb * 14; ld = F_;
        src = (which == 0 ? Wg : Wu);
        wdst = (char*)Wgu_f + (size_t)((nt * 16 + which) * 16 + kb) * 1024;
        wstride = 32 * 1024;
      } else {
        kb = rem >> 2; nt = rem & 3; ld = D_;
        src = Wd;
        wdst = (char*)Wd_f + (size_t)(nt * 8 * 56 + kb) * 1024;
        wstride = 56 * 1024;
      }
      const u16* sp = src + (size_t)(kb * 32 + (lane >> 4)) * ld + nt * 128 + (lane & 15) * 8;
#pragma unroll
      for (int i = 0; i < 8; ++i) rws[i] = *(const u32x4*)(sp + (size_t)(4 * i) * ld);
    }
    if (wtask < 672) {
      char* wl = smem + K1_WOFF + (wave - 4) * (32 * 272);
#pragma unroll
      for (int i = 0; i < 8; ++i) *(u32x4*)(wl + (4 * i + (lane >> 4)) * 272 + (lane & 15) * 16) = rws[i];
      const char* rp = wl + (8 * (lane >> 4) + ((lane & 15) >> 2)) * 272 + (lane & 3) * 8;
#pragma unroll
      for (int j = 0; j < 8; ++j) {
        s16x4 lo = __builtin_amdgcn_ds_read_tr16_b64_v4i16((LDS_PTR s16x4*)(rp + j * 32));
        s16x4 hi = __builtin_amdgcn_ds_read_tr16_b64_v4i16((LDS_PTR s16x4*)(rp + j * 32 + 4 * 272));
        u32x2 l2 = __builtin_bit_cast(u32x2, lo), h2 = __builtin_bit_cast(u32x2, hi);
        *(u32x4*)(wdst + (size_t)j * wstride + lane * 16) = u32x4{l2[0], l2[1], h2[0], h2[1]};
      }
    }
    __builtin_amdgcn_s_barrier();
    return;
  }
  u32x4 g1[2], g0[2], uv[2];
#pragma unroll
  for (int c = 0; c < 2; ++c) {
    const u16* kr = ssmk + (size_t)(d0 + c) * S_;
    int a = 2040 - 8 * tid;
    g1[c] = *(const u32x4*)(kr + a);
    if (a >= 8) g0[c] = *(const u32x4*)(kr + a - 8);
    else g0[c] = u32x4{0, 0, 0, 0};
    uv[c] = *(const u32x4*)(kvT + (size_t)(d0 + c) * S_ + tid * 8);
  }
  const float dval0 = bf2f(ssmD[d0]), dval1 = bf2f(ssmD[d0 + 1]);
  {
    u32x4 z = {0, 0, 0, 0};
    for (int i = tid; i < 2 * 160; i += 256) {
      int c = i / 160, j = i - c * 160;
      *(u32x4*)(smem + K1_UOFF + c * UCH + j * 16) = z;
    }
    if (tid < 64) {
      int c = tid >> 5, r = (tid >> 2) & 7, q = 256 + (tid & 3);
      *(u32x4*)(smem + K1_KOFF + c * KCH + r * KCS + q * 16) = z;
    }
  }
#pragma unroll
  for (int c = 0; c < 2; ++c) {
    u32 W[8], O[7];
#pragma unroll
    for (int i = 0; i < 4; ++i) {
      W[i] = rot16(g1[c][3 - i]);
      W[4 + i] = rot16(g0[c][3 - i]);
    }
#pragma unroll
    for (int i = 0; i < 7; ++i) O[i] = (W[i] >> 16) | (W[i + 1] << 16);
    char* base = smem + K1_KOFF + c * KCH + tid * 16;
#pragma unroll
    for (int r = 0; r < 8; ++r) {
      u32x4 val;
      if ((r & 1) == 0) val = u32x4{W[r / 2], W[r / 2 + 1], W[r / 2 + 2], W[r / 2 + 3]};
      else val = u32x4{O[r / 2], O[r / 2 + 1], O[r / 2 + 2], O[r / 2 + 3]};
      *(u32x4*)(base + r * KCS) = val;
    }
    *(u32x4*)(smem + K1_UOFF + c * UCH + (32 + (tid >> 2)) * UBS + (tid & 3) * 16) = uv[c];
  }
  __syncthreads();

  const int c = wave >> 1, hf = wave & 1;
  const int b = lane & 31, h = lane >> 5;
  f32x16 acc0, acc1;
#pragma unroll
  for (int i = 0; i < 16; ++i) { acc0[i] = 0.f; acc1[i] = 0.f; }
  const int i0 = 2047 + 16 - b + 8 * h;
  const int r = i0 & 7;
  const char* abase = smem + K1_KOFF + c * KCH + r * KCS + (i0 - r) * 2;
  const char* ulo = smem + K1_UOFF + c * UCH + b * UBS + 16 * h;
  const char* uhi = ulo + 32 * UBS;
  constexpr int PD = 3;
  if (hf == 0) {
    const char* ap = abase - 32 * 63;
    bf16x8 af[PD + 1], bl[PD + 1], bh[PD + 1];
#pragma unroll
    for (int j = 0; j < PD; ++j) {
      af[j] = *(const bf16x8*)(ap + 32 * (63 - j));
      bl[j] = *(const bf16x8*)(ulo + k1_boff(j));
      bh[j] = *(const bf16x8*)(uhi + k1_boff(j));
    }
#pragma unroll
    for (int j = 0; j < 64; ++j) {
      if (j + PD < 64) {
        af[(j + PD) % (PD + 1)] = *(const bf16x8*)(ap + 32 * (63 - j - PD));
        bl[(j + PD) % (PD + 1)] = *(const bf16x8*)(ulo + k1_boff(j + PD));
        if (j + PD < 32) bh[(j + PD) % (PD + 1)] = *(const bf16x8*)(uhi + k1_boff(j + PD));
      }
      acc0 = __builtin_amdgcn_mfma_f32_32x32x16_bf16(af[j % (PD + 1)], bl[j % (PD + 1)], acc0, 0, 0, 0);
      if (j < 32) acc1 = __builtin_amdgcn_mfma_f32_32x32x16_bf16(af[j % (PD + 1)], bh[j % (PD + 1)], acc1, 0, 0, 0);
      __builtin_amdgcn_sched_group_barrier(0x8, 1, 0);
      __builtin_amdgcn_sched_group_barrier(0x100, 2, 0);
      __builtin_amdgcn_sched_group_barrier(0x8, 1, 0);
      __builtin_amdgcn_sched_group_barrier(0x100, 1, 0);
      __builtin_amdgcn_sched_barrier(0);
    }
  } else {
    const char* ap = abase - 32 * 127;
    bf16x8 af[PD + 1], bh[PD + 1];
#pragma unroll
    for (int j = 0; j < PD; ++j) {
      af[j] = *(const bf16x8*)(ap + 32 * (127 - 32 - j));
      bh[j] = *(const bf16x8*)(uhi + k1_boff(32 + j));
    }
#pragma unroll
    for (int j = 0; j < 96; ++j) {
      if (j + PD < 96) {
        af[(j + PD) % (PD + 1)] = *(const bf16x8*)(ap + 32 * (127 - 32 - j - PD));
        bh[(j + PD) % (PD + 1)] = *(const bf16x8*)(uhi + k1_boff(32 + j + PD));
      }
      acc0 = __builtin_amdgcn_mfma_f32_32x32x16_bf16(af[j % (PD + 1)], bh[j % (PD + 1)], acc0, 0, 0, 0);
      __builtin_amdgcn_sched_group_barrier(0x8, 1, 0);
      __builtin_amdgcn_sched_group_barrier(0x100, 2, 0);
      __builtin_amdgcn_sched_barrier(0);
    }
  }
  {
    float* xch = (float*)(smem + K1_ROFF + c * 4096);
    if (hf == 0) {
#pragma unroll
      for (int i = 0; i < 4; ++i) {
        f32x4 x;
#pragma unroll
        for (int j = 0; j < 4; ++j) x[j] = acc1[4 * i + j];
        *(f32x4*)(xch + (i * 64 + lane) * 4) = x;
      }
    }
    __syncthreads();
    if (hf == 1) {
#pragma unroll
      for (int i = 0; i < 4; ++i) {
        f32x4 x = *(const f32x4*)(xch + (i * 64 + lane) * 4);
#pragma unroll
        for (int j = 0; j < 4; ++j) acc0[4 * i + j] += x[j];
      }
    }
    const float dval = c ? dval1 : dval0;
    const int A = b + 32 * hf;
    u16* orow = y2T + (size_t)(d0 + c) * 8 + 4 * h;
#pragma unroll
    for (int i = 0; i < 4; ++i) {
      u32x2 kvp = *(const u32x2*)(smem + K1_UOFF + c * UCH + (32 + A) * UBS + (8 * i + 4 * h) * 2);
      float kv[4] = {bflo(kvp[0]), bfhi(kvp[0]), bflo(kvp[1]), bfhi(kvp[1])};
      float o[4];
#pragma unroll
      for (int j = 0; j < 4; ++j) o[j] = rbf(acc0[4 * i + j]) + rbf(kv[j] * dval);
      u32x2 pk = {pack2(o[0], o[1]), pack2(o[2], o[3])};
      *(u32x2*)(orow + (size_t)(4 * A + i) * (D_ * 8)) = pk;
    }
  }
}

template <int CTRL>
__device__ __forceinline__ float dpp_add(float x) {
  int y = __builtin_amdgcn_update_dpp(0, __float_as_int(x), CTRL, 0xf, 0xf, true);
  return x + __int_as_float(y);
}
__device__ __forceinline__ float wave_sum(float x) {
  x = dpp_add<0xB1>(x);
  x = dpp_add<0x4E>(x);
  x = dpp_add<0x141>(x);
  x = dpp_add<0x140>(x);
  int xi = __float_as_int(x);
  float a = __int_as_float(__builtin_amdgcn_readlane(xi, 0));
  float b = __int_as_float(__builtin_amdgcn_readlane(xi, 16));
  float c = __int_as_float(__builtin_amdgcn_readlane(xi, 32));
  float d = __int_as_float(__builtin_amdgcn_readlane(xi, 48));
  return (a + b) + (c + d);
}

__device__ __forceinline__ void ln_bf16(const float (&x)[8], float (&o)[8]) {
  float s = ((x[0] + x[1]) + (x[2] + x[3])) + ((x[4] + x[5]) + (x[6] + x[7]));
  const float mean = wave_sum(s) * (1.f / 512.f);
  float ss = 0.f;
#pragma unroll
  for (int i = 0; i < 8; ++i) { float dlt = x[i] - mean; ss += dlt * dlt; }
  const float var = wave_sum(ss) * (1.f / 512.f);
  const float mb = rbf(mean);
  const float vb = rbf(var);
  const float sd = rbf(sqrtf(rbf(vb + 1e-5f)));
  const float rc = 1.0f / sd;
#pragma unroll
  for (int i = 0; i < 8; ++i) {
    float a = rbf(x[i] - mb);
    float qt = a * rc;
    float rem = __builtin_fmaf(-qt, sd, a);
    qt = __builtin_fmaf(rem, rc, qt);
    o[i] = rbf(qt);
  }
}

#define SGB(mask, n) __builtin_amdgcn_sched_group_barrier(mask, n, 0)
#define AUX_SC1 16

__device__ __forceinline__ void wait_count(const u32* cnt, u32 target) {
  if (threadIdx.x == 0) {
    int spins = 0;
    while (__hip_atomic_load(cnt, __ATOMIC_RELAXED, __HIP_MEMORY_SCOPE_AGENT) < target) {
      __builtin_amdgcn_s_sleep(1);
      if (++spins > (1 << 22)) break;
    }
  }
  __syncthreads();
}
__device__ __forceinline__ void signal_count(u32* cnt) {
  asm volatile("s_waitcnt vmcnt(0)" ::: "memory");
  __syncthreads();
  if (threadIdx.x == 0) __hip_atomic_fetch_add(cnt, 1u, __ATOMIC_RELAXED, __HIP_MEMORY_SCOPE_AGENT);
}

__global__ __launch_bounds__(512) void k234(const u16* __restrict__ q, const u16* __restrict__ y2T,
                                            u16* hbuf, u16* hln_f, const u16* __restrict__ Wgu_f,
                                            u16* hh_f, const u16* __restrict__ Wd_f, u16* __restrict__ out,
                                            u32* cnt) {
  extern __shared__ __attribute__((aligned(16))) char smem[];
  const int tid = threadIdx.x;
  const int wave = tid >> 6, lane = tid & 63;
  const int bid = blockIdx.x;
  const int xcd = bid & 7, idx = bid >> 3;
  const auto rs_h = __builtin_amdgcn_make_buffer_rsrc((void*)hbuf, 0, S_ * D_ * 2, 0x00020000);
  const auto rs_hln = __builtin_amdgcn_make_buffer_rsrc((void*)hln_f, 0, S_ * D_ * 2, 0x00020000);
  const auto rs_hh = __builtin_amdgcn_make_buffer_rsrc((void*)hh_f, 0, S_ * F_ * 2, 0x00020000);
  u32* cnt1 = cnt;
  u32* cnt2 = cnt + 16 * 64;

  {
    u16(*tile)[528] = (u16(*)[528])smem;
    const int s0 = bid * 8;
    u32x4 ya = *(const u32x4*)(y2T + (size_t)bid * (D_ * 8) + tid * 8);
    u32x4 qv = *(const u32x4*)(q + (size_t)(s0 + wave) * D_ + lane * 8);
#pragma unroll
    for (int i = 0; i < 4; ++i) {
      tile[2 * i][tid] = (u16)(ya[i] & 0xffffu);
      tile[2 * i + 1][tid] = (u16)(ya[i] >> 16);
    }
    float x[8], ql[8];
#pragma unroll
    for (int i = 0; i < 4; ++i) {
      x[2 * i] = bflo(qv[i]);
      x[2 * i + 1] = bfhi(qv[i]);
    }
    ln_bf16(x, ql);
    __syncthreads();
    float hh[8], hl[8];
    {
      u32x4 yv = *(const u32x4*)(&tile[wave][lane * 8]);
#pragma unroll
      for (int i = 0; i < 4; ++i) {
        hh[2 * i] = rbf(x[2 * i] + rbf(ql[2 * i] * bflo(yv[i])));
        hh[2 * i + 1] = rbf(x[2 * i + 1] + rbf(ql[2 * i + 1] * bfhi(yv[i])));
      }
    }
    const int s = s0 + wave;
    u32x4 hp = {pack2(hh[0], hh[1]), pack2(hh[2], hh[3]), pack2(hh[4], hh[5]), pack2(hh[6], hh[7])};
    __builtin_amdgcn_raw_buffer_store_b128(hp, rs_h, (s * D_ + lane * 8) * 2, 0, AUX_SC1);
    ln_bf16(hh, hl);
    u32x4 lp = {pack2(hl[0], hl[1]), pack2(hl[2], hl[3]), pack2(hl[4], hl[5]), pack2(hl[6], hl[7])};
    const int off = (((s >> 7) * 16 + (lane >> 2)) * 8 + ((s >> 4) & 7)) * 1024 + ((lane & 3) * 16 + (s & 15)) * 16;
    __builtin_amdgcn_raw_buffer_store_b128(lp, rs_hln, off, 0, AUX_SC1);
    signal_count(cnt1 + (bid >> 4) * 64);
  }

  if (bid < 224) {
    const int ft = (xcd & 1) * 7 + (idx % 7);
    const int rb = (xcd >> 1) * 4 + (idx / 7);
    const char* wbase = (const char*)Wgu_f + (size_t)((ft * 8 + wave) * 2) * 16 * 1024 + lane * 16;
    constexpr int PF = 5;
    bf16x8 wf[PF + 1][2];
#pragma unroll
    for (int p = 0; p < PF; ++p)
#pragma unroll
      for (int j = 0; j < 2; ++j) wf[p][j] = *(const bf16x8*)(wbase + (j * 16 + p) * 1024);
    wait_count(cnt1 + rb * 64, 16);
    const int aoff = rb * 131072 + tid * 16;
    u32x4 ast[2][4];
#pragma unroll
    for (int i = 0; i < 4; ++i) ast[0][i] = __builtin_amdgcn_raw_buffer_load_b128(rs_hln, aoff + i * 8192, 0, AUX_SC1);
#pragma unroll
    for (int i = 0; i < 4; ++i)
      ast[1][i] = __builtin_amdgcn_raw_buffer_load_b128(rs_hln, aoff + 32768 + i * 8192, 0, AUX_SC1);

    f32x4 acc[2][8];
#pragma unroll
    for (int j = 0; j < 2; ++j)
#pragma unroll
      for (int m = 0; m < 8; ++m) acc[j][m] = f32x4{0.f, 0.f, 0.f, 0.f};

    bf16x8 xf[2][8];
#pragma unroll
    for (int ch = 0; ch < 4; ++ch) {
      __builtin_amdgcn_sched_barrier(0);
#pragma unroll
      for (int i = 0; i < 4; ++i) *(u32x4*)(smem + ch * 32768 + i * 8192 + tid * 16) = ast[ch & 1][i];
      if (ch + 2 < 4) {
#pragma unroll
        for (int i = 0; i < 4; ++i)
          ast[ch & 1][i] = __builtin_amdgcn_raw_buffer_load_b128(rs_hln, aoff + (ch + 2) * 32768 + i * 8192, 0, AUX_SC1);
      }
      __syncthreads();
#pragma unroll
      for (int m = 0; m < 8; ++m) xf[0][m] = *(const bf16x8*)(smem + (ch * 32 + m) * 1024 + lane * 16);
      __builtin_amdgcn_sched_barrier(0);
#pragma unroll
      for (int k4 = 0; k4 < 4; ++k4) {
        const int kb = ch * 4 + k4;
        if (kb + PF < 16) {
#pragma unroll
          for (int j = 0; j < 2; ++j)
            wf[(kb + PF) % (PF + 1)][j] = *(const bf16x8*)(wbase + (j * 16 + kb + PF) * 1024);
        }
        if (k4 + 1 < 4) {
#pragma unroll
          for (int m = 0; m < 8; ++m)
            xf[(k4 + 1) & 1][m] = *(const bf16x8*)(smem + ((kb + 1) * 8 + m) * 1024 + lane * 16);
        }
#pragma unroll
        for (int m = 0; m < 8; ++m)
#pragma unroll
          for (int j = 0; j < 2; ++j)
            acc[j][m] = __builtin_amdgcn_mfma_f32_16x16x32_bf16(wf[kb % (PF + 1)][j], xf[k4 & 1][m], acc[j][m], 0, 0, 0);
#pragma unroll
        for (int g = 0; g < 2; ++g) {
          SGB(0x8, 2); SGB(0x100, 1); SGB(0x8, 2); SGB(0x100, 1); SGB(0x8, 2); SGB(0x100, 1); SGB(0x8, 2); SGB(0x100, 1); SGB(0x20, 1);
        }
        __builtin_amdgcn_sched_barrier(0);
      }
    }
    __syncthreads();
    {
      const int qd = lane >> 4, sl = lane & 15;
      char* ob = smem + (wave >> 1) * 1024 + (wave & 1) * 512 + ((qd >> 1) * 16 + sl) * 16 + (qd & 1) * 8;
#pragma unroll
      for (int m = 0; m < 8; ++m) {
        float o[4];
#pragma unroll
        for (int r = 0; r < 4; ++r) {
          float gt = rbf(acc[0][m][r]);
          float up = rbf(acc[1][m][r]);
          gt = gt * (gt > 0.f ? 1.f : 0.f);
          o[r] = gt * up;
        }
        u32x2 pk = {pack2(o[0], o[1]), pack2(o[2], o[3])};
        *(u32x2*)(ob + m * 4096) = pk;
      }
    }
    __syncthreads();
#pragma unroll
    for (int i = 0; i < 4; ++i) {
      const int lo = i * 8192 + tid * 16;
      const int m = lo >> 12, rest = lo & 4095;
      u32x4 v = *(const u32x4*)(smem + lo);
      __builtin_amdgcn_raw_buffer_store_b128(v, rs_hh, ((rb * 8 + m) * 56 + ft * 4) * 1024 + rest, 0, AUX_SC1);
    }
    signal_count(cnt2 + rb * 64);
  }

  {
    const int dt = idx & 7, st = xcd * 4 + (idx >> 3);
    const char* wb = (const char*)Wd_f + (size_t)(dt * 4 * 56 + wave * 7) * 1024 + lane * 16;
    const int xo = (st * 4 * 56 + wave * 7) * 1024 + lane * 16;
    constexpr int PF = 3;
    bf16x8 wf[PF + 1][4], xf[PF + 1][4];
#pragma unroll
    for (int p = 0; p < PF; ++p)
#pragma unroll
      for (int j = 0; j < 4; ++j) wf[p][j] = *(const bf16x8*)(wb + (j * 56 + p) * 1024);
    wait_count(cnt2 + (st >> 1) * 64, 14);
#pragma unroll
    for (int p = 0; p < PF; ++p)
#pragma unroll
      for (int j = 0; j < 4; ++j)
        xf[p][j] = __builtin_bit_cast(bf16x8, __builtin_amdgcn_raw_buffer_load_b128(rs_hh, xo + (j * 56 + p) * 1024, 0, AUX_SC1));
    const int qd = lane >> 4, sl = lane & 15;
    const int ib = wave >> 1, jb = (wave & 1) * 2;
    u32x2 hv[2];
#pragma unroll
    for (int j = 0; j < 2; ++j)
      hv[j] = __builtin_amdgcn_raw_buffer_load_b64(rs_h, ((st * 64 + (jb + j) * 16 + sl) * D_ + dt * 64 + ib * 16 + qd * 4) * 2, 0, AUX_SC1);
    f32x4 acc[4][4];
#pragma unroll
    for (int i = 0; i < 4; ++i)
#pragma unroll
      for (int j = 0; j < 4; ++j) acc[i][j] = f32x4{0.f, 0.f, 0.f, 0.f};
    __builtin_amdgcn_sched_barrier(0);
#pragma unroll
    for (int kb = 0; kb < 7; ++kb) {
      if (kb + PF < 7) {
#pragma unroll
        for (int j = 0; j < 4; ++j) {
          wf[(kb + PF) % (PF + 1)][j] = *(const bf16x8*)(wb + (j * 56 + kb + PF) * 1024);
          xf[(kb + PF) % (PF + 1)][j] = __builtin_bit_cast(
              bf16x8, __builtin_amdgcn_raw_buffer_load_b128(rs_hh, xo + (j * 56 + kb + PF) * 1024, 0, AUX_SC1));
        }
      }
#pragma unroll
      for (int i = 0; i < 4; ++i)
#pragma unroll
        for (int j = 0; j < 4; ++j)
          acc[i][j] = __builtin_amdgcn_mfma_f32_16x16x32_bf16(wf[kb % (PF + 1)][i], xf[kb % (PF + 1)][j], acc[i][j], 0, 0, 0);
#pragma unroll
      for (int g = 0; g < 8; ++g) { SGB(0x8, 2); SGB(0x20, 1); }
      __builtin_amdgcn_sched_barrier(0);
    }
#pragma unroll
    for (int i = 0; i < 4; ++i)
#pragma unroll
      for (int j = 0; j < 4; ++j)
        *(f32x4*)(smem + ((wave * 4 + i) * 4 + j) * 1024 + lane * 16) = acc[i][j];
    __syncthreads();
#pragma unroll
    for (int j = 0; j < 2; ++j) {
      f32x4 sum = f32x4{0.f, 0.f, 0.f, 0.f};
#pragma unroll
      for (int w = 0; w < 8; ++w) sum += *(const f32x4*)(smem + ((w * 4 + ib) * 4 + jb + j) * 1024 + lane * 16);
      float hx[4] = {bflo(hv[j][0]), bfhi(hv[j][0]), bflo(hv[j][1]), bfhi(hv[j][1])};
      float o[4];
#pragma unroll
      for (int r = 0; r < 4; ++r) o[r] = hx[r] + rbf(sum[r]);
      u32x2 pk = {pack2(o[0], o[1]), pack2(o[2], o[3])};
      *(u32x2*)(out + (size_t)(st * 64 + (jb + j) * 16 + sl) * D_ + dt * 64 + ib * 16 + qd * 4) = pk;
    }
  }
}

extern "C" void kernel_launch(void* const* d_in, const int* in_sizes, int n_in, void* d_out, int out_size,
                              void* d_ws, size_t ws_size, hipStream_t stream) {
  const u16* q = (const u16*)d_in[0];
  const u16* k = (const u16*)d_in[1];
  const u16* v = (const u16*)d_in[2];
  const u16* shiftk = (const u16*)d_in[3];
  const u16* ssmk = (const u16*)d_in[4];
  const u16* ssmD = (const u16*)d_in[5];
  const u16* Wg = (const u16*)d_in[7];
  const u16* Wu = (const u16*)d_in[8];
  const u16* Wd = (const u16*)d_in[9];
  char* ws = (char*)d_ws;
  u16* y2T = (u16*)(ws);
  u16* hbuf = (u16*)(ws + (2u << 20));
  u16* hln_f = (u16*)(ws + (4u << 20));
  u16* Wgu_f = (u16*)(ws + (6u << 20));
  u16* Wd_f = (u16*)(ws + (10u << 20));
  u16* hh_f = (u16*)(ws + (12u << 20));
  u16* kvT = (u16*)(ws + (20u << 20));

  u32* cnt = (u32*)(ws + (24u << 20));

  (void)hipFuncSetAttribute((const void*)k1_conv, hipFuncAttributeMaxDynamicSharedMemorySize, K1_LDS);
  (void)hipFuncSetAttribute((const void*)k234, hipFuncAttributeMaxDynamicSharedMemorySize, 131072);

  k0_pre<<<256, 256, 0, stream>>>(k, v, shiftk, kvT, cnt);
  k1_conv<<<256, 448, K1_LDS, stream>>>(kvT, ssmk, ssmD, y2T, Wg, Wu, Wd, Wgu_f, Wd_f);
  k234<<<256, 512, 131072, stream>>>(q, y2T, hbuf, hln_f, Wgu_f, hh_f, Wd_f, (u16*)d_out, cnt);
}
```
